```python
import torch

def _softmax(x, axis=-1):
    return torch.softmax(x, dim=axis)
def _gelu(x):
    return torch.nn.functional.gelu(x, approximate="tanh")
def _silu(x):
    return torch.nn.functional.silu(x)
def _sigmoid(x):
    return torch.sigmoid(x)
def _layernorm(x, axis=-1, eps=1e-5):
    m = x.mean(dim=axis, keepdim=True)
    v = x.var(dim=axis, keepdim=True, unbiased=False)
    return (x - m) / torch.sqrt(v + eps)
def _scatter_add(tgt, idx, src, axis=0):
    return tgt.clone().index_add_(axis, idx, src)
def _segment_sum(src, seg, n):
    out = torch.zeros((n,) + src.shape[1:], dtype=src.dtype)
    return out.index_add_(0, seg, src)
def _pad(x, pw):
    flat = tuple(v for pair in reversed(pw) for v in pair)
    return torch.nn.functional.pad(x, flat)

kernel_name = 'sequential_preLN__sliding_block_cached__glu_gated_bilinear__decode'

def setup_inputs():

    _w = (lambda a: (lambda t: t.to(torch.bfloat16) if t.is_floating_point() else t)(torch.as_tensor(a)))
    def _A_setup():
        B, Sq, D = 2, 1, 512
        Hq, Hkv, Sk, Dh = 32, 8, 32768, 128
        assert Hq % Hkv == 0, (Hq, Hkv)
        return {
            "x": _w((lambda *s: torch.randn(*s, dtype=torch.bfloat16) * 0.05)(B, Sq, D)),
            "k_cache": _w((lambda *s: torch.randn(*s, dtype=torch.bfloat16) * 1.6)(B, Hkv, Sk, Dh)),
            "v_cache": _w((lambda *s: torch.randn(*s, dtype=torch.bfloat16) * 0.8)(B, Hkv, Sk, Dh)),
            "wq": _w((lambda *s: torch.randn(*s, dtype=torch.bfloat16) * 1.6)(D, Hq * Dh) * (D ** -0.5)),
            "wo": _w((lambda *s: torch.randn(*s, dtype=torch.bfloat16) * 0.8)(Hq * Dh, D) * ((Hq * Dh) ** -0.5)),
        }
    _A_in = _A_setup()

    def _B_setup():
        B, S, D, F = 2, 1, 512, 1792
        return {
            "x": _w((lambda *s: torch.randn(*s, dtype=torch.bfloat16) * 0.05)(B, S, D)),
            "w_gate": _w((lambda *s: torch.randn(*s, dtype=torch.bfloat16) * 0.4)(D, F) * (D ** -0.5)),
            "w_up": _w((lambda *s: torch.randn(*s, dtype=torch.bfloat16) * 0.4)(D, F) * (D ** -0.5)),
            "w_down": _w((lambda *s: torch.randn(*s, dtype=torch.bfloat16) * 0.4)(F, D) * (F ** -0.5)),
        }
    _B_in = _B_setup()

    return {
        "A_x": _A_in["x"],
        "A_k_cache": _A_in["k_cache"],
        "A_v_cache": _A_in["v_cache"],
        "A_wq": _A_in["wq"],
        "A_wo": _A_in["wo"],
        "B_x": _B_in["x"],
        "B_w_gate": _B_in["w_gate"],
        "B_w_up": _B_in["w_up"],
        "B_w_down": _B_in["w_down"],
    }

def reference(A_x, A_k_cache, A_v_cache, A_wq, A_wo, B_x, B_w_gate, B_w_up, B_w_down):
    def _A_ref(x, k_cache, v_cache, wq, wo):
        B, Sq, D = x.shape
        Hq, Dh = 32, 128
        Hkv, Sk = k_cache.shape[1], k_cache.shape[2]
        W = min(4096, max(256, Sk // 4))
        rep = Hq // Hkv
        q = (lambda x, *d: x.permute(*d))((x @ wq).reshape(B, Sq, Hq, Dh), 0, 2, 1, 3)
        k = (lambda x, n, axis: torch.repeat_interleave(x, n, dim=axis))(k_cache[:, :, Sk - W:, :], rep, axis=1)
        v = (lambda x, n, axis: torch.repeat_interleave(x, n, dim=axis))(v_cache[:, :, Sk - W:, :], rep, axis=1)
        scores = torch.einsum("bhqd,bhkd->bhqk", q, k) * (Dh ** -0.5)
        p = _softmax(scores, axis=-1)
        o = torch.einsum("bhqk,bhkd->bhqd", p, v)
        return (lambda x, *d: x.permute(*d))(o, 0, 2, 1, 3).reshape(B, Sq, Hq * Dh) @ wo

    def _B_ref(x, w_gate, w_up, w_down):
        g = torch.einsum("bsd,df->bsf", x, w_gate)
        u = torch.einsum("bsd,df->bsf", x, w_up)
        h = g * u
        return torch.einsum("bsf,fd->bsd", h, w_down)

    x = A_x
    h = x + _A_ref(_layernorm(x, axis=-1), A_k_cache, A_v_cache, A_wq, A_wo)
    h2 = h + _B_ref(_layernorm(h, axis=-1), B_w_gate, B_w_up, B_w_down)
    return h2
```

```cpp
#include <hip/hip_runtime.h>
#include <hip/hip_bf16.h>
typedef unsigned short u16;
typedef unsigned v4u __attribute__((ext_vector_type(4)));
__device__ __forceinline__ float bf2f(unsigned b){ return __builtin_bit_cast(float, b<<16); }
__device__ __forceinline__ float bflo(unsigned b){ return __builtin_bit_cast(float, b<<16); }
__device__ __forceinline__ float bfhi(unsigned b){ return __builtin_bit_cast(float, b&0xffff0000u); }
__device__ __forceinline__ unsigned f2bf(float f){ unsigned u=__builtin_bit_cast(unsigned,f); return (u+0x7fffu+((u>>16)&1u))>>16; }
__device__ __forceinline__ float rbf(float f){ return bf2f(f2bf(f)); }
constexpr int D=512,HQ=32,HKV=8,SK=32768,DH=128,W=4096,F=1792;

__device__ float block_sum(float v, float* red){
  for(int o=32;o;o>>=1) v+=__shfl_xor(v,o);
  __syncthreads();
  if((threadIdx.x&63)==0) red[threadIdx.x>>6]=v;
  __syncthreads();
  return red[0]+red[1]+red[2]+red[3];
}
__device__ void ln2(const float* src, float* xn, float* red){
  for(int b=0;b<2;++b){
    float a0=src[b*512+threadIdx.x], a1=src[b*512+256+threadIdx.x];
    float m=block_sum(a0+a1,red)*(1.f/512);
    float d0=a0-m,d1=a1-m;
    float v=block_sum(d0*d0+d1*d1,red)*(1.f/512);
    float r=rsqrtf(v+1e-5f);
    xn[b*512+threadIdx.x]=rbf(d0*r); xn[b*512+256+threadIdx.x]=rbf(d1*r);
  }
  __syncthreads();
}
__global__ void __launch_bounds__(256) k_q(const u16* x,const u16* wq,float* q){
  __shared__ float xs[1024], xn[1024], red[4], part[4][2][64];
  for(int i=threadIdx.x;i<1024;i+=256) xs[i]=bf2f(x[i]);
  __syncthreads();
  ln2(xs,xn,red);
  int c=blockIdx.x*64+(threadIdx.x&63), g=threadIdx.x>>6;
  float a0=0,a1=0;
  for(int r=g*128;r<g*128+128;++r){ float w=bf2f(wq[(size_t)r*4096+c]); a0+=xn[r]*w; a1+=xn[512+r]*w; }
  part[g][0][threadIdx.x&63]=a0; part[g][1][threadIdx.x&63]=a1;
  __syncthreads();
  if(threadIdx.x<128){ int b=threadIdx.x>>6,l=threadIdx.x&63; q[b*4096+blockIdx.x*64+l]=rbf(part[0][b][l]+part[1][b][l]+part[2][b][l]+part[3][b][l]); }
}
__global__ void __launch_bounds__(256) k_attn(const u16* K,const u16* V,const float* q,float* po,float* pml){
  __shared__ float qs[512], p[4][256], red[4];
  int s=blockIdx.x&15, g=blockIdx.x>>4, b=g>>3, kvh=g&7;
  for(int i=threadIdx.x;i<512;i+=256) qs[i]=q[b*4096+kvh*512+i];
  __syncthreads();
  size_t base=((size_t)g*SK+(SK-W)+s*256)*DH;
  const v4u* kr=(const v4u*)(K+base+(size_t)threadIdx.x*DH);
  float sc[4]={0,0,0,0};
  _Pragma("unroll 2") for(int c=0;c<16;++c){ v4u kk=kr[c];
    for(int j=0;j<4;++j){ float k0=bflo(kk[j]),k1=bfhi(kk[j]);
      for(int h=0;h<4;++h) sc[h]+=k0*qs[h*128+c*8+2*j]+k1*qs[h*128+c*8+2*j+1]; } }
  for(int h=0;h<4;++h){
    float v=sc[h]*0.08838834764831845f; float m=v;
    for(int o=32;o;o>>=1) m=fmaxf(m,__shfl_xor(m,o));
    __syncthreads(); if((threadIdx.x&63)==0) red[threadIdx.x>>6]=m; __syncthreads();
    m=fmaxf(fmaxf(red[0],red[1]),fmaxf(red[2],red[3]));
    float e=__expf(v-m); p[h][threadIdx.x]=e;
    float l=block_sum(e,red);
    if(threadIdx.x==0){ pml[(blockIdx.x*4+h)*2]=m; pml[(blockIdx.x*4+h)*2+1]=l; }
  }
  __syncthreads();
  int d=threadIdx.x&127, hh=threadIdx.x>>7;
  float a0=0,a1=0;
  for(int k=0;k<256;++k){ float v=bf2f(V[base+(size_t)k*DH+d]); a0+=p[hh*2][k]*v; a1+=p[hh*2+1][k]*v; }
  po[(blockIdx.x*4+hh*2)*128+d]=a0; po[(blockIdx.x*4+hh*2+1)*128+d]=a1;
}
__global__ void __launch_bounds__(256) k_wo(const float* po,const float* pml,const u16* wo,float* part){
  __shared__ float o[2][128];
  int h=blockIdx.x, kvh=h>>2, hh=h&3;
  { int b=threadIdx.x>>7,d=threadIdx.x&127; int g=b*8+kvh; float M=-1e30f;
    for(int s=0;s<16;++s) M=fmaxf(M,pml[((g*16+s)*4+hh)*2]);
    float L=0,a=0;
    for(int s=0;s<16;++s){ float w=__expf(pml[((g*16+s)*4+hh)*2]-M); L+=w*pml[((g*16+s)*4+hh)*2+1]; a+=w*po[((g*16+s)*4+hh)*128+d]; }
    o[b][d]=rbf(a/L); }
  __syncthreads();
  for(int c=threadIdx.x;c<512;c+=256){ float a0=0,a1=0;
    for(int d=0;d<128;++d){ float w=bf2f(wo[(size_t)(h*128+d)*512+c]); a0+=o[0][d]*w; a1+=o[1][d]*w; }
    part[(h*2+0)*512+c]=a0; part[(h*2+1)*512+c]=a1; }
}
__global__ void __launch_bounds__(256) k_ffn(const u16* x,const float* part,const u16* wg,const u16* wu,const u16* wd,float* hbuf,float* part2){
  __shared__ float hs[1024], xn[1024], red[4], pg[4][2][64], pu[4][2][64], hh[2][64];
  for(int i=threadIdx.x;i<1024;i+=256){ int b=i>>9,c=i&511; float a=0; for(int h=0;h<32;++h) a+=part[(h*2+b)*512+c]; float hv=rbf(bf2f(x[i])+rbf(a)); hs[i]=hv; if(blockIdx.x==0) hbuf[i]=hv; }
  __syncthreads();
  ln2(hs,xn,red);
  int l=threadIdx.x&63,g=threadIdx.x>>6,c=blockIdx.x*64+l;
  float g0=0,g1=0,u0=0,u1=0;
  for(int r=g*128;r<g*128+128;++r){ float a=bf2f(wg[(size_t)r*F+c]),b=bf2f(wu[(size_t)r*F+c]); g0+=xn[r]*a; g1+=xn[512+r]*a; u0+=xn[r]*b; u1+=xn[512+r]*b; }
  pg[g][0][l]=g0;pg[g][1][l]=g1;pu[g][0][l]=u0;pu[g][1][l]=u1;
  __syncthreads();
  if(threadIdx.x<128){ int b=threadIdx.x>>6; float gg=rbf(pg[0][b][l]+pg[1][b][l]+pg[2][b][l]+pg[3][b][l]); float uu=rbf(pu[0][b][l]+pu[1][b][l]+pu[2][b][l]+pu[3][b][l]); hh[b][l]=rbf(gg*uu); }
  __syncthreads();
  for(int cc=threadIdx.x;cc<512;cc+=256){ float a0=0,a1=0;
    for(int f=0;f<64;++f){ float w=bf2f(wd[(size_t)(blockIdx.x*64+f)*512+cc]); a0+=hh[0][f]*w; a1+=hh[1][f]*w; }
    part2[(blockIdx.x*2+0)*512+cc]=a0; part2[(blockIdx.x*2+1)*512+cc]=a1; }
}
__global__ void __launch_bounds__(256) k_out(const float* hbuf,const float* part2,u16* out){
  int i=blockIdx.x*256+threadIdx.x; int b=i>>9,c=i&511; float a=0;
  for(int j=0;j<28;++j) a+=part2[(j*2+b)*512+c];
  out[i]=f2bf(hbuf[i]+rbf(a));
}
extern "C" void kernel_launch(void* const* d_in, const int* in_sizes, int n_in, void* d_out, int out_size, void* d_ws, size_t ws_size, hipStream_t stream){
  const u16* x=(const u16*)d_in[0]; const u16* K=(const u16*)d_in[1]; const u16* V=(const u16*)d_in[2];
  const u16* wq=(const u16*)d_in[3]; const u16* wo=(const u16*)d_in[4];
  const u16* wg=(const u16*)d_in[6]; const u16* wu=(const u16*)d_in[7]; const u16* wd=(const u16*)d_in[8];
  float* ws=(float*)d_ws; float* q=ws; float* po=ws+8192; float* pml=po+256*512; float* part=pml+256*8; float* hbuf=part+32*1024; float* part2=hbuf+1024;
  k_q<<<64,256,0,stream>>>(x,wq,q);
  k_attn<<<256,256,0,stream>>>(K,V,q,po,pml);
  k_wo<<<32,256,0,stream>>>(po,pml,wo,part);
  k_ffn<<<28,256,0,stream>>>(x,part,wg,wu,wd,hbuf,part2);
  k_out<<<4,256,0,stream>>>(hbuf,part2,(u16*)d_out);
}
```

```cpp
#include <hip/hip_runtime.h>
#include <hip/hip_bf16.h>

typedef unsigned short u16;
typedef unsigned v4u __attribute__((ext_vector_type(4)));
typedef unsigned v2u __attribute__((ext_vector_type(2)));
typedef float f32x4 __attribute__((ext_vector_type(4)));
typedef float f32x2 __attribute__((ext_vector_type(2)));
typedef short s16x4 __attribute__((ext_vector_type(4)));
typedef __bf16 bf16x8 __attribute__((ext_vector_type(8)));
typedef __bf16 bf16x2 __attribute__((ext_vector_type(2)));
#define GAS __attribute__((address_space(1)))
#define LAS __attribute__((address_space(3)))

#define QSLEEP 8

constexpr unsigned SPIN_CAP = 1u << 17;
constexpr unsigned WS_QP = 4096;
constexpr unsigned WS_ML = WS_QP + 8 * 2 * 4 * 512 * 8;
constexpr unsigned WS_HP = WS_ML + 32 * 2 * 16 * 16;
constexpr unsigned WS_AP = WS_HP + 8 * 4096;
constexpr unsigned WS_BP = WS_AP + 128 * 8192;
constexpr unsigned WS_CP = WS_BP + 128 * 8192;
constexpr unsigned WS_END = WS_CP + 128 * 8192;
constexpr unsigned L_K = 0, L_V = 65536, L_WQ = 131072, L_XN = 147456, L_Q = L_XN + 2048, L_P = L_Q + 4 * 272, L_MX = L_P + 4 * 528,
                   L_LS = L_MX + 64, L_TAG = L_LS + 64, L_O = L_TAG + 16, L_H = L_O + 128, L_GU = L_H + 2 * 1040, L_CS = L_GU + 1024,
                   L_ST = L_CS + 512, L_X = L_ST + 16, L_END = L_X + 2048;

#define STAMP(i) do {} while (0)
#define RET() return
#define LDS_EXTRA 0

struct Params { const u16 *x, *K, *V, *wq, *wo, *wg, *wu, *wd; u16* out; unsigned char* ws; };

__device__ __forceinline__ float bflo(unsigned v) { return __builtin_bit_cast(float, v << 16); }
__device__ __forceinline__ float bfhi(unsigned v) { return __builtin_bit_cast(float, v & 0xffff0000u); }
__device__ __forceinline__ unsigned pk(float lo, float hi) { f32x2 v = {lo, hi}; return __builtin_bit_cast(unsigned, __builtin_convertvector(v, bf16x2)); }
__device__ __forceinline__ float rb(float f) { return bflo(pk(f, f)); }
__device__ __forceinline__ float asf(unsigned u) { return __builtin_bit_cast(float, u); }
__device__ __forceinline__ unsigned asu(float f) { return __builtin_bit_cast(unsigned, f); }
template <int CTRL> __device__ __forceinline__ float dpp(float x) {
  return __builtin_bit_cast(float, __builtin_amdgcn_mov_dpp(__builtin_bit_cast(int, x), CTRL, 0xf, 0xf, true));
}
template <int CTRL> __device__ __forceinline__ unsigned dppu(unsigned x) { return (unsigned)__builtin_amdgcn_mov_dpp((int)x, CTRL, 0xf, 0xf, true); }
constexpr int ROR1 = 0x121, ROR2 = 0x122, ROR4 = 0x124, ROR8 = 0x128, XOR2 = 0x4E;
__device__ __forceinline__ float xrow_sum(float x) {
  auto s = __builtin_amdgcn_permlane16_swap(asu(x), asu(x), false, false);
  x = asf(s[0]) + asf(s[1]);
  auto t = __builtin_amdgcn_permlane32_swap(asu(x), asu(x), false, false);
  return asf(t[0]) + asf(t[1]);
}
__device__ __forceinline__ float xrow_max(float x) {
  auto s = __builtin_amdgcn_permlane16_swap(asu(x), asu(x), false, false);
  x = fmaxf(asf(s[0]), asf(s[1]));
  auto t = __builtin_amdgcn_permlane32_swap(asu(x), asu(x), false, false);
  return fmaxf(asf(t[0]), asf(t[1]));
}
__device__ __forceinline__ float sum_mod4(float x) { x += dpp<ROR4>(x); x += dpp<ROR8>(x); return xrow_sum(x); }
__device__ __forceinline__ float wave_sum(float x) { x += dpp<ROR1>(x); x += dpp<ROR2>(x); return sum_mod4(x); }
__device__ __forceinline__ f32x4 mfma32(v4u a, v4u b, f32x4 c) {
  return __builtin_amdgcn_mfma_f32_16x16x32_bf16(__builtin_bit_cast(bf16x8, a), __builtin_bit_cast(bf16x8, b), c, 0, 0, 0);
}
__device__ __forceinline__ f32x4 mfma16(v2u a, v2u b, f32x4 c) {
  return __builtin_amdgcn_mfma_f32_16x16x16bf16_1k(__builtin_bit_cast(s16x4, a), __builtin_bit_cast(s16x4, b), c, 0, 0, 0);
}
__device__ __forceinline__ v2u trread(LAS unsigned char* a) { return __builtin_bit_cast(v2u, __builtin_amdgcn_ds_read_tr16_b64_v4i16((LAS s16x4*)a)); }
__device__ __forceinline__ v4u cat(v2u a, v2u b) { return v4u{a.x, a.y, b.x, b.y}; }
__device__ __forceinline__ float sel4(int g, float a, float b, float c, float d) {
  const bool b0 = g & 1, b1 = g & 2;
  const float ab = b0 ? b : a, cd = b0 ? d : c;
  return b1 ? cd : ab;
}
template <int N, int STRIDE>
__device__ __forceinline__ unsigned sweep(__amdgpu_buffer_rsrc_t rs, unsigned voff, unsigned soff, unsigned tag, v4u (&v)[N]) {
#pragma unroll
  for (int t = 0; t < N; ++t) v[t] = __builtin_amdgcn_raw_buffer_load_b128(rs, voff + ((t * STRIDE) & 4095), soff + ((t * STRIDE) & ~4095), 16);
  unsigned aa = tag, oo = tag;
#pragma unroll
  for (int t = 0; t < N; ++t) { aa &= v[t].y & v[t].w; oo |= v[t].y | v[t].w; }
  return aa ^ oo;
}
template <int N, int STRIDE>
__device__ __forceinline__ void gather(__amdgpu_buffer_rsrc_t rs, unsigned voff, unsigned soff, unsigned tag, v4u (&v)[N]) {
  for (unsigned spin = 0; spin < SPIN_CAP; ++spin) {
    asm volatile("" ::: "memory");
    if (!__any(sweep<N, STRIDE>(rs, voff, soff, tag, v) != 0u)) break;
  }
}
__device__ __forceinline__ void dma(const void* sbase, unsigned voff, unsigned ldsaddr) {
  unsigned keep;
  asm volatile("s_mov_b32 %0, m0\n\ts_mov_b32 m0, %3\n\ts_nop 0\n\tglobal_load_lds_dwordx4 %1, %2 nt\n\ts_mov_b32 m0, %0"
               : "=&s"(keep) : "v"(voff), "s"(sbase), "s"(ldsaddr) : "memory");
}
#define BARRIER() asm volatile("s_waitcnt lgkmcnt(0)\n\ts_barrier" ::: "memory")
#define VMWAIT(n) asm volatile("s_waitcnt vmcnt(" #n ")" ::: "memory")
constexpr int SC1 = 16;

__global__ void __launch_bounds__(512, 2) fused_decode(Params p) {
  extern __shared__ __attribute__((aligned(16))) unsigned char smem[];
  LAS unsigned char* L = (LAS unsigned char*)smem;
  const unsigned lb = (unsigned)(size_t)L;
  const int tid = threadIdx.x, lane = tid & 63, w = __builtin_amdgcn_readfirstlane(tid >> 6), c = blockIdx.x;
  const int x8 = c & 7, j = c >> 3, b = j >> 4, s = j & 15;
  const int g = lane >> 4, n = lane & 15, q4 = n >> 2, pp = lane & 3;
  const __amdgpu_buffer_rsrc_t rs = __builtin_amdgcn_make_buffer_rsrc((void*)p.ws, 0, (int)WS_END, 0x00020000);
  const bool is_wo = j < 16, is_ffn = (j >= 16) && (x8 < 7);
  const int a = x8 * 16 + (j & 15);
  unsigned tag;
  unsigned stmask = 0; (void)stmask;

  if (w >= 4) {
    const int i = w - 4;
    if (w == 4) STAMP(0);
    const unsigned rot = (unsigned)(2 * j + x8 + i);
    const size_t kvb = ((size_t)(b * 8 + x8) * 32768 + 28672 + s * 256) * 128;
    const unsigned voK = g * 256 + ((n ^ (4 * (rot & 3) + g)) << 4), voV = g * 256 + ((n ^ ((g << 2) | (rot & 3))) << 4);
    asm volatile("s_barrier" ::: "memory");
#pragma unroll 4
    for (int t = 0; t < 16; ++t) { const unsigned pc = ((rot + 4 * t) & 63) << 10; dma(p.K + kvb, voK + pc, lb + L_K + pc); }
#pragma unroll 4
    for (int t = 0; t < 16; ++t) { const unsigned pc = ((rot + 4 * t) & 63) << 10; dma(p.V + kvb, voV + pc, lb + L_V + pc); }
    if (w == 4) STAMP(4);
    VMWAIT(16);
    BARRIER();
    tag = *(LAS unsigned*)(L + L_TAG);
    BARRIER();
    if (is_wo) {
      const unsigned vo = g * 1024 + ((n ^ (g << 2)) << 4);
#pragma unroll
      for (int t = 0; t < 8; ++t) dma(p.wo + (size_t)a * 32 * 512 + i * 128, (vo ^ ((t & 3) << 4)) + t * 4096, lb + L_K + i * 8192 + t * 1024);
      VMWAIT(8);
    } else if (is_ffn) {
      const unsigned vo = (lane >> 1) * 3584 + (lane & 1) * 16;
#pragma unroll
      for (int t = 0; t < 4; ++t) dma(p.wg + (size_t)i * 128 * 1792 + a * 16, vo + t * 32 * 3584, lb + L_K + i * 12288 + t * 1024);
#pragma unroll
      for (int t = 0; t < 4; ++t) dma(p.wu + (size_t)i * 128 * 1792 + a * 16, vo + t * 32 * 3584, lb + L_K + i * 12288 + 4096 + t * 1024);
      const unsigned vd = g * 1024 + ((n ^ (g << 2)) << 4);
#pragma unroll
      for (int t = 0; t < 4; ++t) dma(p.wd + (size_t)a * 16 * 512 + i * 128, (vd ^ (t << 4)) + t * 4096, lb + L_K + i * 12288 + 8192 + t * 1024);
      VMWAIT(12);
    } else {
      VMWAIT(0);
    }
    if (w == 4) STAMP(6);
    BARRIER();
  } else {
    __builtin_amdgcn_s_setprio(2);
    if (w == 0) {
      const int rg = j & 3, cg = j >> 2;
      unsigned e; v4u x0, x1;
      asm volatile("global_load_dword %0, %1, %2" : "=v"(e) : "v"(0u), "s"(p.ws) : "memory");
      asm volatile("global_load_dwordx4 %0, %1, %2" : "=v"(x0) : "v"(lane * 16), "s"(p.x) : "memory");
      asm volatile("global_load_dwordx4 %0, %1, %2 offset:1024" : "=v"(x1) : "v"(lane * 16), "s"(p.x) : "memory");
      const u16* wqb = p.wq + (size_t)rg * 128 * 4096 + x8 * 512 + cg * 64;
      const unsigned vq = (lane >> 3) * 8192 + (((lane & 7) ^ ((g & 1) << 1)) << 4);
#pragma unroll
      for (int t = 0; t < 16; ++t) { const unsigned pc = (t + 2 * cg) & 15; dma(wqb, (vq ^ ((t & 1) << 6)) + pc * 65536, lb + L_WQ + pc * 1024); }
      asm volatile("s_barrier" ::: "memory");
      asm volatile("s_waitcnt vmcnt(16)" : "+v"(e), "+v"(x0), "+v"(x1)::"memory");
      tag = __builtin_amdgcn_readfirstlane(e) + 1u;
#pragma unroll
      for (int bb = 0; bb < 2; ++bb) {
        const v4u xv = bb ? x1 : x0;
        float f[8];
#pragma unroll
        for (int k = 0; k < 4; ++k) { f[2 * k] = bflo(xv[k]); f[2 * k + 1] = bfhi(xv[k]); }
        float sm = ((f[0] + f[1]) + (f[2] + f[3])) + ((f[4] + f[5]) + (f[6] + f[7]));
        const float mean = wave_sum(sm) * (1.f / 512.f);
        float sq = 0.f;
#pragma unroll
        for (int k = 0; k < 8; ++k) { const float d = f[k] - mean; sq = fmaf(d, d, sq); }
        const float var = wave_sum(sq) * (1.f / 512.f);
        const float mb = rb(mean), den = rb(__builtin_sqrtf(rb(rb(var) + 1e-5f))), rc = __builtin_amdgcn_rcpf(den);
        v4u o;
#pragma unroll
        for (int k = 0; k < 4; ++k) {
          const unsigned dd = pk(f[2 * k] - mb, f[2 * k + 1] - mb);
          const float d0 = bflo(dd), d1 = bfhi(dd);
          float q0 = d0 * rc, q1 = d1 * rc;
          q0 = fmaf(fmaf(-q0, den, d0), rc, q0); q1 = fmaf(fmaf(-q1, den, d1), rc, q1);
          o[k] = pk(q0, q1);
        }
        *(LAS v4u*)(L + L_XN + bb * 1024 + lane * 16) = o;
        *(LAS v4u*)(L + L_X + bb * 1024 + lane * 16) = xv;
      }
      if (lane == 0) *(LAS unsigned*)(L + L_TAG) = tag;
      v4u xa[4];
#pragma unroll
      for (int ks = 0; ks < 4; ++ks) xa[ks] = *(LAS v4u*)(L + L_XN + (n & 1) * 1024 + rg * 256 + ks * 64 + g * 16);
      STAMP(1);
      VMWAIT(0);
      f32x4 d[4];
      const unsigned fq = (((q4 >> 1) & 1) | ((g & 1) << 1)) << 1;
#pragma unroll
      for (int T = 0; T < 4; ++T) {
        LAS unsigned char* base = L + L_WQ + (8 * g + q4) * 128 + ((((2 * T) ^ fq) + (pp >> 1)) << 4) + 8 * (pp & 1);
        d[T] = f32x4{0.f, 0.f, 0.f, 0.f};
#pragma unroll
        for (int ks = 0; ks < 4; ++ks) d[T] = mfma32(xa[ks], cat(trread(base + ks * 4096), trread(base + ks * 4096 + 512)), d[T]);
      }
      const float q0 = sel4(g, d[0][0], d[1][0], d[2][0], d[3][0]), q1 = sel4(g, d[0][1], d[1][1], d[2][1], d[3][1]);
      const unsigned qo = WS_QP + ((x8 * 2 * 4 + rg) * 512 + cg * 64 + lane) * 8;
      __builtin_amdgcn_raw_buffer_store_b64(v2u{asu(q0), tag}, rs, qo, 0, SC1);
      __builtin_amdgcn_raw_buffer_store_b64(v2u{asu(q1), tag}, rs, qo + 4 * 512 * 8, 0, SC1);
      STAMP(2);
      __builtin_amdgcn_s_sleep(QSLEEP);
      v4u v[16];
      gather<16, 1024>(rs, lane * 16, WS_QP + (x8 * 2 + b) * 4 * 512 * 8, tag, v);
#pragma unroll
      for (int h = 0; h < 4; ++h) {
        const float s0 = (asf(v[h].x) + asf(v[4 + h].x)) + (asf(v[8 + h].x) + asf(v[12 + h].x));
        const float s1 = (asf(v[h].z) + asf(v[4 + h].z)) + (asf(v[8 + h].z) + asf(v[12 + h].z));
        *(LAS unsigned*)(L + L_Q + h * 272 + lane * 4) = pk(s0, s1);
      }
      STAMP(3);
    } else {
      asm volatile("s_barrier" ::: "memory");
    }
    BARRIER();
    tag = *(LAS unsigned*)(L + L_TAG);
    v4u qf[4];
#pragma unroll
    for (int ks = 0; ks < 4; ++ks) qf[ks] = *(LAS v4u*)(L + L_Q + pp * 272 + (4 * ks + g) * 16);
    f32x4 d[4];
#pragma unroll
    for (int u = 0; u < 4; ++u) {
      d[u] = f32x4{0.f, 0.f, 0.f, 0.f};
#pragma unroll
      for (int ks = 0; ks < 4; ++ks) {
        const v4u kf = *(LAS v4u*)(L + L_K + (64 * w + 16 * u + n) * 256 + (((4 * ks + g) ^ n) << 4));
        d[u] = mfma32(kf, qf[ks], d[u]);
      }
    }
    float sc[4];
#pragma unroll
    for (int r = 0; r < 4; ++r) sc[r] = sel4(q4, d[0][r], d[1][r], d[2][r], d[3][r]);
    {
      const unsigned a0 = pk(sc[0], sc[1]), a1 = pk(sc[2], sc[3]);
      const float SCALE = 0.08838834764831845f;
      const unsigned b0 = pk(bflo(a0) * SCALE, bfhi(a0) * SCALE), b1 = pk(bflo(a1) * SCALE, bfhi(a1) * SCALE);
      sc[0] = bflo(b0); sc[1] = bfhi(b0); sc[2] = bflo(b1); sc[3] = bfhi(b1);
    }
    float mx = fmaxf(fmaxf(sc[0], sc[1]), fmaxf(sc[2], sc[3]));
    mx = fmaxf(mx, dpp<ROR4>(mx)); mx = fmaxf(mx, dpp<ROR8>(mx)); mx = xrow_max(mx);
    if (lane < 4) *(LAS float*)(L + L_MX + (lane * 4 + w) * 4) = mx;
    BARRIER();
    {
      const f32x4 m4 = *(LAS f32x4*)(L + L_MX + pp * 16);
      const float m = fmaxf(fmaxf(m4[0], m4[1]), fmaxf(m4[2], m4[3]));
      const float LOG2E = 1.4426950408889634f;
      float pr[4];
#pragma unroll
      for (int r = 0; r < 4; ++r) pr[r] = __builtin_amdgcn_exp2f((sc[r] - m) * LOG2E);
      *(LAS v2u*)(L + L_P + pp * 528 + (64 * w + 16 * q4 + 4 * g) * 2) = v2u{pk(pr[0], pr[1]), pk(pr[2], pr[3])};
      const float ls = sum_mod4((pr[0] + pr[1]) + (pr[2] + pr[3]));
      if (lane < 4) *(LAS float*)(L + L_LS + (lane * 4 + w) * 4) = ls;
    }
    if (w == 0) STAMP(5);
    BARRIER();
  }

  {
    LAS unsigned char* pb = L + L_P + pp * 528 + g * 16;
    LAS unsigned char* v0 = L + L_V + (8 * g + q4) * 256 + (((2 * w + (pp >> 1)) ^ ((q4 << 2) | ((2 * g) & 3))) << 4) + 8 * (pp & 1);
    LAS unsigned char* v1 = L + L_V + (8 * g + 4 + q4) * 256 + (((2 * w + (pp >> 1)) ^ ((q4 << 2) | ((2 * g + 1) & 3))) << 4) + 8 * (pp & 1);
    f32x4 o0 = {0.f, 0.f, 0.f, 0.f}, o1 = {0.f, 0.f, 0.f, 0.f};
#pragma unroll
    for (int ks = 0; ks < 8; ks += 2) {
      o0 = mfma32(*(LAS v4u*)(pb + ks * 64), cat(trread(v0 + ks * 8192), trread(v1 + ks * 8192)), o0);
      o1 = mfma32(*(LAS v4u*)(pb + ks * 64 + 64), cat(trread(v0 + ks * 8192 + 8192), trread(v1 + ks * 8192 + 8192)), o1);
    }
    o0 += o1;
    const float val = sel4(g, o0[0], o0[1], o0[2], o0[3]);
    __builtin_amdgcn_raw_buffer_store_b64(v2u{asu(val), tag}, rs,
        WS_AP + (((x8 * 16 + 4 * g + (w >> 1)) * 2 + b) * 16 + s) * 256 + ((w & 1) * 16 + n) * 8, 0, SC1);
  }
  if (w == 3) {
    if (lane < 4) {
      const f32x4 m4 = *(LAS f32x4*)(L + L_MX + lane * 16), l4 = *(LAS f32x4*)(L + L_LS + lane * 16);
      const float m = fmaxf(fmaxf(m4[0], m4[1]), fmaxf(m4[2], m4[3])), l = (l4[0] + l4[1]) + (l4[2] + l4[3]);
      __builtin_amdgcn_raw_buffer_store_b128(v4u{asu(m), tag, asu(l), tag}, rs, WS_ML + (((x8 * 4 + lane) * 2 + b) * 16 + s) * 16, 0, SC1);
    }
    RET();
  }
  if (w == 0) STAMP(7);
  if (w == 2) RET();

  if (is_wo) {
    if (w >= 4) {
      const int i = w - 4;
      VMWAIT(1);
      if (w == 4) STAMP(8);
      v4u wf[8];
      LAS unsigned char* bw = L + L_K + i * 8192 + (8 * g + q4) * 256 + 8 * (pp & 1);
#pragma unroll
      for (int T = 0; T < 8; ++T)
        wf[T] = cat(trread(bw + (((2 * T + (pp >> 1)) ^ ((q4 << 2) | ((2 * g) & 3))) << 4)),
                    trread(bw + 1024 + (((2 * T + (pp >> 1)) ^ ((q4 << 2) | ((2 * g + 1) & 3))) << 4)));
      BARRIER();
      const v4u of = *(LAS v4u*)(L + L_O + (n & 1) * 64 + g * 16);
      f32x4 d[8];
#pragma unroll
      for (int T = 0; T < 8; ++T) d[T] = mfma32(of, wf[T], f32x4{0.f, 0.f, 0.f, 0.f});
#pragma unroll
      for (int u = 0; u < 2; ++u) {
        const float r0 = sel4(g, d[4 * u][0], d[4 * u + 1][0], d[4 * u + 2][0], d[4 * u + 3][0]);
        const float r1 = sel4(g, d[4 * u][1], d[4 * u + 1][1], d[4 * u + 2][1], d[4 * u + 3][1]);
        const int col = 128 * i + 64 * u + lane;
        __builtin_amdgcn_raw_buffer_store_b128(v4u{asu(r0), tag, asu(r1), tag}, rs, WS_BP + (col >> 2) * 8192 + a * 64 + (col & 3) * 16, 0, SC1);
      }
      if (w == 4) STAMP(10);
      RET();
    }
    const float xr0 = bflo(*(LAS u16*)(L + L_X + (4 * a + pp) * 2)), xr1 = bflo(*(LAS u16*)(L + L_X + 1024 + (4 * a + pp) * 2));
    {
      v4u v[4], ml[4];
      for (unsigned spin = 0; spin < SPIN_CAP; ++spin) {
        asm volatile("" ::: "memory");
        const unsigned bad = sweep<4, 1024>(rs, lane * 16, WS_AP + (a * 2 + w) * 4096, tag, v) | sweep<4, 64>(rs, g * 16, WS_ML + ((a >> 2) * 2 + w) * 256, tag, ml);
        if (!__any(bad != 0u)) break;
      }
      const float M = xrow_max(fmaxf(fmaxf(asf(ml[0].x), asf(ml[1].x)), fmaxf(asf(ml[2].x), asf(ml[3].x))));
      float Ls = 0.f, a0 = 0.f, a1 = 0.f;
#pragma unroll
      for (int t = 0; t < 4; ++t) {
        const float wt = __builtin_amdgcn_exp2f((asf(ml[t].x) - M) * 1.4426950408889634f);
        Ls = fmaf(wt, asf(ml[t].z), Ls); a0 = fmaf(wt, asf(v[t].x), a0); a1 = fmaf(wt, asf(v[t].z), a1);
      }
      Ls = xrow_sum(Ls); a0 = xrow_sum(a0); a1 = xrow_sum(a1);
      const float inv = __builtin_amdgcn_rcpf(Ls);
      if (lane < 16) *(LAS unsigned*)(L + L_O + w * 64 + lane * 4) = pk(a0 * inv, a1 * inv);
    }
    if (w == 0) STAMP(9);
    BARRIER();
    if (w == 1) RET();
    float h0, h1;
    {
      v4u v[8];
      gather<8, 1024>(rs, lane * 16, WS_BP + a * 8192, tag, v);
      float s0 = ((asf(v[0].x) + asf(v[1].x)) + (asf(v[2].x) + asf(v[3].x))) + ((asf(v[4].x) + asf(v[5].x)) + (asf(v[6].x) + asf(v[7].x)));
      float s1 = ((asf(v[0].z) + asf(v[1].z)) + (asf(v[2].z) + asf(v[3].z))) + ((asf(v[4].z) + asf(v[5].z)) + (asf(v[6].z) + asf(v[7].z)));
      s0 = sum_mod4(s0); s1 = sum_mod4(s1);
      const unsigned ar = pk(s0, s1);
      const unsigned hp = pk(xr0 + bflo(ar), xr1 + bfhi(ar));
      h0 = bflo(hp); h1 = bfhi(hp);
      const unsigned c0 = dppu<0x00>(hp), c1 = dppu<0x55>(hp), c2 = dppu<0xAA>(hp), c3 = dppu<0xFF>(hp);
      const unsigned sl = (lane & 1) ? 0x07060302u : 0x05040100u;
      if (lane < 14)
        __builtin_amdgcn_raw_buffer_store_b128(v4u{__builtin_amdgcn_perm(c1, c0, sl), tag, __builtin_amdgcn_perm(c3, c2, sl), tag}, rs,
                                               WS_HP + lane * 2048 + a * 16, 0, SC1);
    }
    STAMP(11);
    {
      v4u v[7];
      gather<7, 1024>(rs, lane * 16, WS_CP + a * 8192, tag, v);
      float s0 = ((asf(v[0].x) + asf(v[1].x)) + (asf(v[2].x) + asf(v[3].x))) + ((asf(v[4].x) + asf(v[5].x)) + asf(v[6].x));
      float s1 = ((asf(v[0].z) + asf(v[1].z)) + (asf(v[2].z) + asf(v[3].z))) + ((asf(v[4].z) + asf(v[5].z)) + asf(v[6].z));
      s0 = sum_mod4(s0); s1 = sum_mod4(s1);
      const unsigned dr = pk(s0, s1);
      const unsigned op = pk(h0 + bflo(dr), h1 + bfhi(dr));
      const unsigned c0 = dppu<0x00>(op), c1 = dppu<0x55>(op), c2 = dppu<0xAA>(op), c3 = dppu<0xFF>(op);
      const unsigned sl = (lane & 1) ? 0x07060302u : 0x05040100u;
      if (lane < 2) *(GAS v2u*)(p.out + lane * 512 + 4 * a) = v2u{__builtin_amdgcn_perm(c1, c0, sl), __builtin_amdgcn_perm(c3, c2, sl)};
      if (c == 0 && lane == 0) __builtin_amdgcn_raw_buffer_store_b32(tag, rs, 0, 0, SC1);
    }
    STAMP(15);
    RET();
  }
  if (!is_ffn || w == 1) RET();
  if (w >= 4) {
    const int i = w - 4;
    VMWAIT(1);
    if (w == 4) STAMP(8);
    v4u ag[4], au[4]; v2u wdf[8];
    LAS unsigned char* bg = L + L_K + i * 12288 + (8 * g + q4) * 32 + pp * 8;
#pragma unroll
    for (int ks = 0; ks < 4; ++ks) {
      ag[ks] = cat(trread(bg + ks * 1024), trread(bg + ks * 1024 + 128));
      au[ks] = cat(trread(bg + 4096 + ks * 1024), trread(bg + 4096 + ks * 1024 + 128));
    }
    LAS unsigned char* bd = L + L_K + i * 12288 + 8192 + (4 * g + q4) * 256 + 8 * (pp & 1);
#pragma unroll
    for (int T = 0; T < 8; ++T) wdf[T] = trread(bd + (((2 * T + (pp >> 1)) ^ ((q4 << 2) | g)) << 4));
    {
      const v4u ones = {0x3F803F80u, 0x3F803F80u, 0x3F803F80u, 0x3F803F80u};
      f32x4 cg_ = {0.f, 0.f, 0.f, 0.f}, cu_ = {0.f, 0.f, 0.f, 0.f};
#pragma unroll
      for (int ks = 0; ks < 4; ++ks) { cg_ = mfma32(ag[ks], ones, cg_); cu_ = mfma32(au[ks], ones, cu_); }
      if (n < 2) *(LAS f32x4*)(L + L_CS + (i * 2 + n) * 64 + g * 16) = n ? cu_ : cg_;
    }
    BARRIER();
    f32x4 G = {0.f, 0.f, 0.f, 0.f}, U = {0.f, 0.f, 0.f, 0.f};
#pragma unroll
    for (int ks = 0; ks < 4; ++ks) {
      const v4u hf = *(LAS v4u*)(L + L_H + (n & 1) * 1040 + (128 * i + 32 * ks + 8 * g) * 2);
      G = mfma32(ag[ks], hf, G); U = mfma32(au[ks], hf, U);
    }
    f32x4 cs = *(LAS f32x4*)(L + L_CS + (n >> 1) * 64 + g * 16);
#pragma unroll
    for (int r = 0; r < 4; ++r) { cs[r] += dpp<ROR4>(cs[r]); cs[r] += dpp<ROR8>(cs[r]); }
    if (n < 4) *(LAS f32x4*)(L + L_GU + (i * 4 + n) * 64 + g * 16) = (n & 2) ? U : G;
    BARRIER();
    f32x4 t4 = *(LAS f32x4*)(L + L_GU + n * 64 + g * 16);
    const f32x2 st = *(LAS f32x2*)(L + L_ST + (n & 1) * 8);
    float hh[4];
#pragma unroll
    for (int r = 0; r < 4; ++r) {
      float t = t4[r]; t += dpp<ROR4>(t); t += dpp<ROR8>(t);
      t = fmaf(-st[0], cs[r], t) * st[1];
      hh[r] = t * dpp<XOR2>(t);
    }
    const v2u ha = {pk(hh[0], hh[1]), pk(hh[2], hh[3])};
    f32x4 d[8];
#pragma unroll
    for (int T = 0; T < 8; ++T) d[T] = mfma16(ha, wdf[T], f32x4{0.f, 0.f, 0.f, 0.f});
#pragma unroll
    for (int u = 0; u < 2; ++u) {
      const float r0 = sel4(g, d[4 * u][0], d[4 * u + 1][0], d[4 * u + 2][0], d[4 * u + 3][0]);
      const float r1 = sel4(g, d[4 * u][1], d[4 * u + 1][1], d[4 * u + 2][1], d[4 * u + 3][1]);
      const int col = 128 * i + 64 * u + lane;
      __builtin_amdgcn_raw_buffer_store_b128(v4u{asu(r0), tag, asu(r1), tag}, rs, WS_CP + (col >> 2) * 8192 + a * 64 + (col & 3) * 16, 0, SC1);
    }
    if (w == 4) STAMP(13);
    RET();
  }
  {
    v4u v[4];
    gather<4, 1024>(rs, lane * 16, WS_HP + x8 * 4096, tag, v);
#pragma unroll
    for (int t = 0; t < 4; ++t) *(LAS v2u*)(L + L_H + (t >> 1) * 1040 + (t & 1) * 512 + lane * 8) = v2u{v[t].x, v[t].z};
    STAMP(12);
    BARRIER();
#pragma unroll
    for (int bb = 0; bb < 2; ++bb) {
      const unsigned u0 = v[2 * bb].x, u1 = v[2 * bb].z, u2 = v[2 * bb + 1].x, u3 = v[2 * bb + 1].z;
      const float f[8] = {bflo(u0), bfhi(u0), bflo(u1), bfhi(u1), bflo(u2), bfhi(u2), bflo(u3), bfhi(u3)};
      const float mean = wave_sum(((f[0] + f[1]) + (f[2] + f[3])) + ((f[4] + f[5]) + (f[6] + f[7]))) * (1.f / 512.f);
      float sq = 0.f;
#pragma unroll
      for (int k = 0; k < 8; ++k) { const float d = f[k] - mean; sq = fmaf(d, d, sq); }
      const float var = wave_sum(sq) * (1.f / 512.f);
      if (lane == 0) *(LAS f32x2*)(L + L_ST + bb * 8) = f32x2{mean, __builtin_amdgcn_rsqf(var + 1e-5f)};
    }
    BARRIER();
  }
  RET();
}

extern "C" void kernel_launch(void* const* d_in, const int* in_sizes, int n_in, void* d_out, int out_size, void* d_ws, size_t ws_size,
                              hipStream_t stream) {
  Params p{(const u16*)d_in[0], (const u16*)d_in[1], (const u16*)d_in[2], (const u16*)d_in[3], (const u16*)d_in[4],
           (const u16*)d_in[6], (const u16*)d_in[7], (const u16*)d_in[8], (u16*)d_out, (unsigned char*)d_ws};
  (void)hipFuncSetAttribute((const void*)fused_decode, hipFuncAttributeMaxDynamicSharedMemorySize, (int)L_END + LDS_EXTRA);
  fused_decode<<<256, 512, L_END + LDS_EXTRA, stream>>>(p);
}
```

```cpp
#include <hip/hip_runtime.h>
#include <hip/hip_bf16.h>

typedef unsigned short u16;
typedef unsigned v4u __attribute__((ext_vector_type(4)));
typedef unsigned v2u __attribute__((ext_vector_type(2)));
typedef float f32x4 __attribute__((ext_vector_type(4)));
typedef float f32x2 __attribute__((ext_vector_type(2)));
typedef short s16x4 __attribute__((ext_vector_type(4)));
typedef __bf16 bf16x8 __attribute__((ext_vector_type(8)));
typedef __bf16 bf16x2 __attribute__((ext_vector_type(2)));
#define GAS __attribute__((address_space(1)))
#define LAS __attribute__((address_space(3)))

#define QDELAY 320
#define RJ 2
#define RX 8
#define KDELAY 60
#define FDELAY 100
#define QHINT 1
#define QMAX 600
#define THR 8
#define WT 4
#define THRW() asm volatile("s_waitcnt vmcnt(%0)" :: "i"(THR - 1) : "memory")
#define WTHR() asm volatile("s_waitcnt vmcnt(%0)" :: "i"(WT - 1) : "memory")
#define SB() __builtin_amdgcn_sched_barrier(0)
constexpr unsigned SPIN_CAP = 1u << 17;
constexpr unsigned EMPTY = 0xFFFFFFFFu;
constexpr unsigned WS_FL = 1024;
constexpr unsigned WS_QP = 4096;
constexpr unsigned QST = 9216;
constexpr unsigned WS_ML = WS_QP + 16 * QST;
constexpr unsigned WS_AP = WS_ML + 16 * 2048;
constexpr unsigned WS_BP = WS_AP + 256 * 1024;
constexpr unsigned WS_CP = WS_BP + 8 * 16384;
constexpr unsigned WS_END = WS_CP + 16 * 4096;
constexpr unsigned PST = 656;
constexpr unsigned SST = 1056;
constexpr unsigned L_K = 0, L_V = 65536, L_STG = 131072  ,
                   L_Q = L_STG + 16 * SST, L_P = L_Q + 4 * 272, L_MX = L_P + 4 * PST, L_LS = L_MX + 64, L_XN = L_LS + 64, L_CTL = L_XN + 1024, L_W = L_CTL + 32,
                   L_GU = L_W + 512, L_ST = L_GU + 4096, L_HH = L_ST + 128, L_END = L_HH + 2 * 144;
constexpr unsigned C_KI = 0, C_QR = 4, C_KR = 8, C_WI = 12, C_QP = 16, C_GT = 20;

#define STAMP(i) do {} while (0)
#define STAMP0() do {} while (0)
#define RET() return
#define LDS_EXTRA 0

struct Params { const u16 *x, *K, *V, *wq, *wo, *wg, *wu, *wd; u16* out; unsigned char* ws; };

__device__ __forceinline__ float bflo(unsigned v) { return __builtin_bit_cast(float, v << 16); }
__device__ __forceinline__ float bfhi(unsigned v) { return __builtin_bit_cast(float, v & 0xffff0000u); }
__device__ __forceinline__ unsigned pk(float lo, float hi) { f32x2 v = {lo, hi}; return __builtin_bit_cast(unsigned, __builtin_convertvector(v, bf16x2)); }
__device__ __forceinline__ float rb(float f) { return bflo(pk(f, f)); }
__device__ __forceinline__ float asf(unsigned u) { return __builtin_bit_cast(float, u); }
__device__ __forceinline__ unsigned asu(float f) { return __builtin_bit_cast(unsigned, f); }
__device__ __forceinline__ unsigned ne(unsigned u) { return u < EMPTY ? u : EMPTY - 1u; }
template <int CTRL> __device__ __forceinline__ float dpp(float x) {
  return __builtin_bit_cast(float, __builtin_amdgcn_mov_dpp(__builtin_bit_cast(int, x), CTRL, 0xf, 0xf, true));
}
constexpr int ROR1 = 0x121, ROR2 = 0x122, ROR4 = 0x124, ROR8 = 0x128, XOR1 = 0xB1, XOR2 = 0x4E, HMIR = 0x141;
__device__ __forceinline__ float sw16(float a, float b) {
  auto s = __builtin_amdgcn_permlane16_swap(asu(a), asu(b), false, false); return asf(s[0]) + asf(s[1]);
}
__device__ __forceinline__ float sw32(float a, float b) {
  auto s = __builtin_amdgcn_permlane32_swap(asu(a), asu(b), false, false); return asf(s[0]) + asf(s[1]);
}
__device__ __forceinline__ float xrow_sum(float x) { x = sw16(x, x); return sw32(x, x); }
__device__ __forceinline__ float xrow_max(float x) {
  auto s = __builtin_amdgcn_permlane16_swap(asu(x), asu(x), false, false);
  x = fmaxf(asf(s[0]), asf(s[1]));
  auto t = __builtin_amdgcn_permlane32_swap(asu(x), asu(x), false, false);
  return fmaxf(asf(t[0]), asf(t[1]));
}
__device__ __forceinline__ float sum_mod4(float x) { x += dpp<ROR4>(x); x += dpp<ROR8>(x); return xrow_sum(x); }
__device__ __forceinline__ float max_mod4(float x) { x = fmaxf(x, dpp<ROR4>(x)); x = fmaxf(x, dpp<ROR8>(x)); return xrow_max(x); }
__device__ __forceinline__ float wave_sum(float x) { x += dpp<ROR1>(x); x += dpp<ROR2>(x); return sum_mod4(x); }
__device__ __forceinline__ f32x4 mfma32(v4u a, v4u b, f32x4 c) {
  return __builtin_amdgcn_mfma_f32_16x16x32_bf16(__builtin_bit_cast(bf16x8, a), __builtin_bit_cast(bf16x8, b), c, 0, 0, 0);
}
__device__ __forceinline__ float dot2(unsigned a, unsigned b, float c) {
  return __builtin_amdgcn_fdot2_f32_bf16(__builtin_bit_cast(bf16x2, a), __builtin_bit_cast(bf16x2, b), c, false);
}
__device__ __forceinline__ v2u trread(LAS unsigned char* a) { return __builtin_bit_cast(v2u, __builtin_amdgcn_ds_read_tr16_b64_v4i16((LAS s16x4*)a)); }
__device__ __forceinline__ v4u cat(v2u a, v2u b) { return v4u{a.x, a.y, b.x, b.y}; }
__device__ __forceinline__ float sel4(int g, float a, float b, float c, float d) {
  const bool b0 = g & 1, b1 = g & 2;
  const float ab = b0 ? b : a, cd = b0 ? d : c;
  return b1 ? cd : ab;
}
__device__ __forceinline__ unsigned umax(unsigned a, unsigned b) { return a > b ? a : b; }
constexpr int SC1 = 16;
template <int N>
__device__ __forceinline__ void gather_tag(__amdgpu_buffer_rsrc_t rs, unsigned voff, unsigned soff, unsigned tag, v4u (&v)[N]) {
  _Pragma("nounroll") for (unsigned spin = 0; spin < SPIN_CAP; ++spin) {
    asm volatile("" ::: "memory");
#pragma unroll
    for (int t = 0; t < N; ++t) v[t] = __builtin_amdgcn_raw_buffer_load_b128(rs, voff + ((t * 1024) & 4095), soff + ((t * 1024) & ~4095), SC1);
    unsigned aa = tag, oo = tag;
#pragma unroll
    for (int t = 0; t < N; ++t) { aa &= v[t].y & v[t].w; oo |= v[t].y | v[t].w; }
    if (!__any(aa != oo)) break;
  }
}
template <int N>
__device__ __forceinline__ void gather_dense(__amdgpu_buffer_rsrc_t rs, unsigned voff, unsigned soff, v4u (&v)[N]) {
  _Pragma("nounroll") for (unsigned spin = 0; spin < SPIN_CAP; ++spin) {
    asm volatile("" ::: "memory");
#pragma unroll
    for (int t = 0; t < N; ++t) v[t] = __builtin_amdgcn_raw_buffer_load_b128(rs, voff + t * 1024, soff, SC1);
    unsigned mx = 0;
#pragma unroll
    for (int t = 0; t < N; ++t) mx = umax(umax(umax(mx, v[t].x), v[t].y), umax(v[t].z, v[t].w));
    if (!__any(mx == EMPTY)) break;
  }
}
__device__ __forceinline__ void dma(const void* sbase, unsigned voff, unsigned ldsaddr) {
  unsigned keep;
  asm volatile("s_mov_b32 %0, m0\n\ts_mov_b32 m0, %3\n\ts_nop 0\n\tglobal_load_lds_dwordx4 %1, %2 nt\n\ts_mov_b32 m0, %0"
               : "=&s"(keep) : "v"(voff), "s"(sbase), "s"(ldsaddr) : "memory");
}
__device__ __forceinline__ unsigned long long rt() { return __builtin_amdgcn_s_memrealtime(); }
#define BARRIER() asm volatile("s_waitcnt lgkmcnt(0)\n\ts_barrier" ::: "memory")
#define VMWAIT(n) asm volatile("s_waitcnt vmcnt(" #n ")" ::: "memory")
#define PIN(x) asm volatile("" : "+v"(x))
__device__ __forceinline__ unsigned tagdiff(v4u v, unsigned tag) { return (v.y ^ tag) | (v.w ^ tag); }
__device__ __forceinline__ unsigned flagdiff(v4u v, unsigned tag) { return ((v.x ^ tag) | (v.y ^ tag)) | ((v.z ^ tag) | (v.w ^ tag)); }
__device__ __forceinline__ void round_scores(float (&sc)[4]) {
  const unsigned a0 = pk(sc[0], sc[1]), a1 = pk(sc[2], sc[3]);
  const float SCALE = 0.08838834764831845f;
  const unsigned b0 = pk(bflo(a0) * SCALE, bfhi(a0) * SCALE), b1 = pk(bflo(a1) * SCALE, bfhi(a1) * SCALE);
  sc[0] = bflo(b0); sc[1] = bfhi(b0); sc[2] = bflo(b1); sc[3] = bfhi(b1);
}
constexpr float LOG2E = 1.4426950408889634f;

__device__ __forceinline__ void lds_inc(LAS unsigned char* a, int lane) {
  if (lane == 0) __hip_atomic_fetch_add((LAS unsigned*)a, 1u, __ATOMIC_RELAXED, __HIP_MEMORY_SCOPE_WORKGROUP);
}
__device__ __forceinline__ void lds_wait(LAS unsigned char* a, unsigned want) {
  _Pragma("nounroll") for (unsigned spin = 0; spin < (SPIN_CAP << 4) && *(volatile LAS unsigned*)a < want; ++spin) __builtin_amdgcn_s_sleep(1);
  asm volatile("" ::: "memory");
}

__device__ __forceinline__ unsigned wmax(v4u v) { return umax(umax(v.x, v.y), umax(v.z, v.w)); }
__device__ __forceinline__ void wait_ticks(unsigned long long t0, unsigned ticks) { while (rt() - t0 < ticks) __builtin_amdgcn_s_sleep(1); }

__global__ void __launch_bounds__(512, 2) fused_decode(Params p) {
  unsigned keep0, keep1;
  asm volatile("s_load_dword %0, %2, 0x0\n\ts_load_dword %1, %2, 0x40" : "=&s"(keep0), "=&s"(keep1) : "s"(__builtin_amdgcn_kernarg_segment_ptr()) : "memory");
  extern __shared__ __attribute__((aligned(16))) unsigned char smem[];
  LAS unsigned char* L = (LAS unsigned char*)smem;
  const unsigned lb = (unsigned)(size_t)L;
  const int tid = threadIdx.x, lane = tid & 63, w = __builtin_amdgcn_readfirstlane(tid >> 6), c = blockIdx.x;
  const int x8 = c & 7, j = c >> 3, b = j >> 4, s = j & 15;
  const int g = lane >> 4, n = lane & 15, q4 = n >> 2, pp = lane & 3;
  const bool is_wo = s < 4, q_half = (j & 0x16) == 0x14, has_q = (unsigned)(s - 4) < 8u || q_half, is_ded = s >= 14, is_ffn = is_ded && x8 < 7;
  const int grp = b * 8 + x8, fd = x8 * 4 + b * 2 + (s & 1), rg = s & 1;
  const int nks = j == 0 ? 4 : (s < 4 + 2 * b ? 8 : 10), ks0 = b ? (s < 6 ? 8 * s : 10 * s - 12) : (s < 4 ? max(8 * s - 4, 0) : 10 * s - 12);
  const bool ten = nks == 10;
  const f32x4 Z4 = {0.f, 0.f, 0.f, 0.f};
  const v4u ONES = {0x3F803F80u, 0x3F803F80u, 0x3F803F80u, 0x3F803F80u};
  const v4u E4 = {EMPTY, EMPTY, EMPTY, EMPTY};
  unsigned stmask = 0; (void)stmask;
  const unsigned long long t0 = rt();
  if (tid < 8) *(LAS unsigned*)(L + L_CTL + tid * 4) = 0u;
#define RS() __builtin_amdgcn_make_buffer_rsrc((void*)p.ws, 0, (int)WS_END, 0x00020000)
#define ARGS() asm volatile("s_waitcnt lgkmcnt(0)" :: "s"(keep0), "s"(keep1) : "memory")
#define B0() asm volatile("s_barrier" ::: "memory")
#define RAISE_FLAG(k) do { if (tid == 448) __builtin_amdgcn_raw_buffer_store_b64(v2u{ep + 1u, ep + 1u}, rs, 0, WS_FL + (k) * 8, SC1); } while (0)
#define LOAD_EP() unsigned ep; asm volatile("global_load_dword %0, %1, %2" : "=v"(ep) : "v"(0u), "s"(p.ws) : "memory")

  if (is_ded) {
    ARGS();
    if (w == 4) { STAMP0(); }
    if (!is_ffn) RET();
    const __amdgpu_buffer_rsrc_t rs = RS();
    if (w == 7) {
      __builtin_amdgcn_raw_buffer_store_b128(E4, rs, (lane >> 3) * 4096 + (lane & 7) * 16, WS_CP + fd * 128, SC1);
      __builtin_amdgcn_raw_buffer_store_b128(E4, rs, (lane >> 3) * 4096 + (lane & 7) * 16, WS_CP + 32768 + fd * 128, SC1);
    }
    LOAD_EP();
    asm volatile("s_waitcnt vmcnt(0)" : "+v"(ep)::"memory");
    RAISE_FLAG(64 + fd);
    wait_ticks(t0, FDELAY);
    const int kq = w & 3, fh = w >> 2;
    const unsigned fq = (((q4 >> 1) & 1) | ((g & 1) << 1)) << 1;
    v4u gf[8], uf[8], df[8];
    {
      const unsigned sw = ((lane & 7) ^ ((g & 1) << 1)) << 4, vd = (lane >> 3) * 1024 + sw, vo = (lane >> 3) * 3584 + sw;
      const u16* db = p.wd + (size_t)fd * 64 * 512 + w * 64;
      const u16* gb = p.wg + (size_t)w * 64 * 1792 + fd * 64;
      const u16* ub = p.wu + (size_t)w * 64 * 1792 + fd * 64;
#pragma unroll
      for (int t = 0; t < 8; ++t) {
        dma(db, (vd ^ ((t & 1) << 6)) + t * 8192, lb + w * 8192 + t * 1024);
        dma(ub, (vo ^ ((t & 1) << 6)) + t * 8 * 3584, lb + 65536 + w * 8192 + t * 1024);
      }
      VMWAIT(1);
#pragma unroll
      for (int T = 0; T < 4; ++T) {
        LAS unsigned char* base = L + w * 8192 + (8 * g + q4) * 128 + ((((2 * T) ^ fq) + (pp >> 1)) << 4) + 8 * (pp & 1);
        df[2 * T] = cat(trread(base), trread(base + 512));
        df[2 * T + 1] = cat(trread(base + 4096), trread(base + 4096 + 512));
      }
      asm volatile("s_waitcnt lgkmcnt(0)" : "+v"(df[0]), "+v"(df[1]), "+v"(df[2]), "+v"(df[3]), "+v"(df[4]), "+v"(df[5]), "+v"(df[6]), "+v"(df[7])::"memory");
#pragma unroll
      for (int t = 0; t < 8; ++t) dma(gb, (vo ^ ((t & 1) << 6)) + t * 8 * 3584, lb + w * 8192 + t * 1024);
      VMWAIT(0);
    }
    BARRIER();
    const unsigned tag = __builtin_amdgcn_readfirstlane(ep) + 1u;
    unsigned gw_ = L_GU + (2 * fh * 64 + lane) * 16 + kq * 4, gr_ = L_GU + (kq * 64 + lane) * 16;
    {
#pragma unroll
      for (int T = 0; T < 2; ++T) {
        LAS unsigned char* base = L + kq * 16384 + (8 * g + q4) * 128 + ((((4 * fh + 2 * T) ^ fq) + (pp >> 1)) << 4) + 8 * (pp & 1);
#pragma unroll
        for (int k = 0; k < 4; ++k) {
          LAS unsigned char* a = base + ((k ^ (2 * fh)) << 12);
          gf[T * 4 + k] = cat(trread(a), trread(a + 512));
          uf[T * 4 + k] = cat(trread(a + 65536), trread(a + 65536 + 512));
        }
        f32x4 cg_ = Z4, cu_ = Z4;
#pragma unroll
        for (int k = 0; k < 4; ++k) { cg_ = mfma32(ONES, gf[T * 4 + k], cg_); cu_ = mfma32(ONES, uf[T * 4 + k], cu_); }
        *(LAS float*)(L + gw_ + T * 1024) = (g & 1) ? cu_[0] : cg_[0];
      }
    }
    BARRIER();
    float cs;
    { const f32x4 a = *(LAS f32x4*)(L + gr_); cs = (a[0] + a[1]) + (a[2] + a[3]); }
    PIN(cs);
#pragma unroll
    for (int k = 0; k < 8; ++k) { PIN(gf[k]); PIN(uf[k]); }
    if (w == 4) STAMP(8);
    _Pragma("nounroll") for (unsigned spin = 0; spin < SPIN_CAP; ++spin) {
      asm volatile("" ::: "memory");
      const v4u f0 = __builtin_amdgcn_raw_buffer_load_b128(rs, (lane & 31) * 16, WS_FL, SC1);
      const v4u ml = __builtin_amdgcn_raw_buffer_load_b128(rs, (lane < 56 ? lane >> 2 : 0) * 128 + pp * 16, WS_ML + grp * 2048, SC1);
      if (!__any((flagdiff(f0, tag) | tagdiff(ml, tag)) != 0u)) break;
      __builtin_amdgcn_s_sleep(8);
    }
    unsigned sw_ = L_STG + 2 * w * SST + lane * 16, sr0 = L_STG + n * SST + kq * 256 + fh * 128 + g * 16, sr1 = sr0 + 128 - fh * 256;
    unsigned tw_ = L_ST + g * 32 + w * 4, tr_ = L_ST + g * 32, hw_ = L_HH + (g >> 1) * 144 + (16 * kq + n) * 2, hr_ = L_HH + (n & 1) * 144 + g * 16;
    unsigned co_ = n < 4 ? (g >> 1) * 4096 + (g & 1) * 64 + n * 16 : 0x80000000u;
    PIN(sw_); PIN(sr0); PIN(sr1); PIN(gw_); PIN(gr_); PIN(tw_); PIN(tr_); PIN(hw_); PIN(hr_); PIN(co_);
    {
      v4u v[2];
      gather_dense<2>(rs, lane * 16, WS_BP + x8 * 16384 + 2 * w * 1024, v);
      *(LAS v4u*)(L + sw_) = v[0]; *(LAS v4u*)(L + sw_ + SST) = v[1];
    }
    if (w == 0) STAMP(12);
    BARRIER();
    if (w == 5) STAMP(1);
    {
      const v4u h0 = *(LAS v4u*)(L + sr0), h1 = *(LAS v4u*)(L + sr0 + 64), h2 = *(LAS v4u*)(L + sr1), h3 = *(LAS v4u*)(L + sr1 + 64);
      SB();
      f32x4 Q = mfma32(h0, h0, Z4), S = mfma32(h0, ONES, Z4);
      Q = mfma32(h1, h1, Q); S = mfma32(h1, ONES, S);
      SB();
      f32x4 G0 = mfma32(h0, gf[0], Z4), U0 = mfma32(h0, uf[0], Z4);
      G0 = mfma32(h1, gf[1], G0); U0 = mfma32(h1, uf[1], U0);
      float q = (Q[0] + Q[1]) + (Q[2] + Q[3]);
      G0 = mfma32(h2, gf[2], G0); U0 = mfma32(h2, uf[2], U0);
      q += dpp<XOR1>(q); q += dpp<XOR2>(q); q += dpp<HMIR>(q);
      G0 = mfma32(h3, gf[3], G0); U0 = mfma32(h3, uf[3], U0);
      const float st = sw16((S[0] + S[1]) + (S[2] + S[3]), q);
      if (n == (g >> 1) * 8) *(LAS float*)(L + tw_) = st;
      SB();
      f32x4 G1 = mfma32(h0, gf[4], Z4), U1 = mfma32(h0, uf[4], Z4);
      G1 = mfma32(h1, gf[5], G1); U1 = mfma32(h1, uf[5], U1);
      *(LAS float*)(L + gw_) = sw16((G0[0] + G0[1]) + (G0[2] + G0[3]), (U0[0] + U0[1]) + (U0[2] + U0[3]));
      G1 = mfma32(h2, gf[6], G1); U1 = mfma32(h2, uf[6], U1);
      G1 = mfma32(h3, gf[7], G1); U1 = mfma32(h3, uf[7], U1);
      *(LAS float*)(L + gw_ + 1024) = sw16((G1[0] + G1[1]) + (G1[2] + G1[3]), (U1[0] + U1[1]) + (U1[2] + U1[3]));
    }
    if (w == 6) STAMP(2);
    BARRIER();
    if (w == 7) STAMP(3);
    if (w < 4) {
      const f32x4 s0 = *(LAS f32x4*)(L + tr_), s1 = *(LAS f32x4*)(L + tr_ + 16), a = *(LAS f32x4*)(L + gr_);
      SB();
      const float so = (((s0[0] + s0[1]) + (s0[2] + s0[3])) + ((s1[0] + s1[1]) + (s1[2] + s1[3]))) * (1.f / 512.f);
      auto sx = __builtin_amdgcn_permlane16_swap(asu(so), asu(so), false, false);
      const float mean = asf(sx[0]);
      const float r2 = __builtin_amdgcn_rcpf(fmaxf(fmaf(-mean, mean, asf(sx[1])), 0.f) + 1e-5f);
      const float t = fmaf(-mean, cs, (a[0] + a[1]) + (a[2] + a[3]));
      auto tx = __builtin_amdgcn_permlane16_swap(asu(t), asu(t), false, false);
      const float hh = asf(tx[0]) * asf(tx[1]) * r2;
      *(LAS u16*)(L + hw_) = (u16)pk(hh, hh);
    }
    BARRIER();
    if (w == 7) STAMP(4);
    {
      const v4u b0 = *(LAS v4u*)(L + hr_), b1 = *(LAS v4u*)(L + hr_ + 64);
      f32x4 d[4];
#pragma unroll
      for (int T = 0; T < 4; ++T) d[T] = mfma32(df[2 * T], b0, Z4);
#pragma unroll
      for (int T = 0; T < 4; ++T) d[T] = mfma32(df[2 * T + 1], b1, d[T]);
      const bool th = n & 2;
      const unsigned a0 = pk(d[0][0], d[0][1]), a1 = pk(d[0][2], d[0][3]), a2 = pk(d[1][0], d[1][1]), a3 = pk(d[1][2], d[1][3]);
      const unsigned c0 = pk(d[2][0], d[2][1]), c1 = pk(d[2][2], d[2][3]), c2 = pk(d[3][0], d[3][1]), c3 = pk(d[3][2], d[3][3]);
      __builtin_amdgcn_raw_buffer_store_b128(v4u{ne(th ? c0 : a0), ne(th ? c1 : a1), ne(th ? c2 : a2), ne(th ? c3 : a3)}, rs, co_,
                                                        WS_CP + 2 * w * 4096 + fd * 128, SC1);
    }
    if (w == 4) STAMP(11);
    RET();
  }

  const unsigned np = 8 * nks, rot = (((RJ * j + RX * x8) & 63) * np) >> 6;
  const unsigned kvb = (unsigned)(grp * 32768 + 28672 + ks0 * 32) * 128u;
#define PIECE(beta) (((rot + (beta)) < np ? rot + (beta) : rot + (beta) - np) << 10)
  if (w >= 4) {
    const int i = w - 4;
    const unsigned voV = g * 256 + ((n ^ ((g << 2) | i)) << 4);
#define VPIECE(t) dma(p.V + kvb, voV + PIECE(4 * (t) + i), lb + L_V + (4 * (t) + i) * 1024)
    v4u wf[16];
    unsigned xres = 0;
    int cg;
    const unsigned fw = ((2 * g + (q4 >> 1)) & 3) << 1, wsw = ((lane & 7) ^ (g << 1)) << 4;
    LAS unsigned char* base = L + L_V + 16384 + i * 1024 + (g >> 1) * 4096 + (4 * (g & 1) + q4) * 128 + (pp >> 1) * 16 + 8 * (pp & 1);
    if (is_wo) {
      ARGS(); B0();
      if (w == 4) { STAMP0(); }
      const __amdgpu_buffer_rsrc_t rs = RS();
      cg = (3 - (int)((size_t)p.wo >> 7) + b * 4 + s) & 7;
      if (w == 7) {
        __builtin_amdgcn_raw_buffer_store_b128(E4, rs, lane * 16, WS_AP + (grp * 16 + s) * 1024, SC1);
        __builtin_amdgcn_raw_buffer_store_b128(E4, rs, (lane >> 3) * 16384 + (lane & 7) * 16, WS_BP + x8 * 1024 + cg * 128, SC1);
        __builtin_amdgcn_raw_buffer_store_b128(E4, rs, (lane >> 3) * 16384 + (lane & 7) * 16, WS_BP + 8192 + x8 * 1024 + cg * 128, SC1);
      }
      LOAD_EP();
      asm volatile("global_load_dword %0, %1, %2" : "=v"(xres) : "v"((lane >> 5) * 1024 + cg * 128 + (lane & 31) * 4), "s"(p.x) : "memory");
      const u16* wb = p.wo + (size_t)(x8 * 512 + i * 128) * 512 + cg * 64;
      const unsigned vow = (lane >> 3) * 1024 + wsw;
#define WPIECE(t) dma(wb, vow + (t) * 8192, lb + L_V + 16384 + (4 * (t) + i) * 1024)
#define WFRAGS(ks) do { _Pragma("unroll") for (int T = 0; T < 4; ++T) { LAS unsigned char* a = base + (((2 * T) ^ fw) << 4) + (ks) * 16384; \
          wf[T * 4 + (ks)] = cat(trread(a), trread(a + 8192)); } \
        asm volatile("s_waitcnt lgkmcnt(0)" : "+v"(wf[ks]), "+v"(wf[4 + (ks)]), "+v"(wf[8 + (ks)]), "+v"(wf[12 + (ks)])::"memory"); } while (0)
      asm volatile("s_waitcnt vmcnt(0)" : "+v"(ep), "+v"(xres)::"memory");
      RAISE_FLAG(x8 * 8 + cg);
      lds_wait(L + L_CTL + C_KI, 4u);
#pragma unroll 4
      for (int t = 0; t < 16; ++t) { THRW(); WPIECE(t); }
      THRW(); VPIECE(0); THRW(); VPIECE(1); THRW(); VPIECE(2); THRW(); VPIECE(3);
      VMWAIT(8);
      WFRAGS(0); WFRAGS(1); WFRAGS(2);
      for (int t = 4; t < 2 * nks; ++t) { THRW(); VPIECE(t); }
      if (w == 4) STAMP(4);
      VMWAIT(8);
      WFRAGS(3);
    } else {
      if (has_q) {
        v4u x0, x1; unsigned y;
        const int r0 = rg * 256 + i * 64 + (s >= 12 ? 32 : 0);
        unsigned xo = (lane >> 5) * 1024 + (lane & 31) * 16, yo = (lane >> 5) * 1024 + ((r0 * 2 + (lane & 31) * 4) & 1023);
        unsigned vow = (r0 + (lane >> 3)) * 8192 + x8 * 1024 + wsw;
        PIN(xo); PIN(yo); PIN(vow);
        ARGS();
        cg = (3 - (int)((size_t)p.wq >> 7) + (s < 12 ? (1 - b) * 4 + ((s - 4) >> 1) : 0)) & 7;
        asm volatile("global_load_dwordx4 %0, %1, %2" : "=v"(x0) : "v"(xo), "s"(p.x) : "memory");
        asm volatile("global_load_dwordx4 %0, %1, %2 offset:512" : "=v"(x1) : "v"(xo), "s"(p.x) : "memory");
        asm volatile("global_load_dword %0, %1, %2" : "=v"(y) : "v"(yo), "s"(p.x) : "memory");
#pragma unroll
        for (int t = 0; t < 4; ++t) dma(p.wq + cg * 64, vow + t * 65536, lb + L_V + 16384 + (4 * t + i) * 1024);
        if (!q_half) {
#pragma unroll
          for (int t = 4; t < 8; ++t) dma(p.wq + cg * 64, vow + t * 65536, lb + L_V + 16384 + (4 * t + i) * 1024);
        }
        LOAD_EP();
        B0();
        if (w == 4) { STAMP0(); }
        const __amdgpu_buffer_rsrc_t rs = RS();
        __builtin_amdgcn_s_setprio(2);
        if (q_half) VMWAIT(5);
        asm volatile("s_waitcnt vmcnt(9)" : "+v"(x0), "+v"(x1), "+v"(y)::"memory");
        float sm = 0.f, sq = 0.f;
#pragma unroll
        for (int k = 0; k < 4; ++k) { sm = dot2(x0[k], 0x3F803F80u, sm); sq = dot2(x0[k], x0[k], sq); sm = dot2(x1[k], 0x3F803F80u, sm); sq = dot2(x1[k], x1[k], sq); }
        sm += dpp<ROR1>(sm); sq += dpp<ROR1>(sq); sm += dpp<ROR2>(sm); sq += dpp<ROR2>(sq);
        sm += dpp<ROR4>(sm); sq += dpp<ROR4>(sq); sm += dpp<ROR8>(sm); sq += dpp<ROR8>(sq);
        const float mean = sw16(sm, sm) * (1.f / 512.f), var = fmaxf(fmaf(-mean, mean, sw16(sq, sq) * (1.f / 512.f)), 0.f);
        const float mb = rb(mean), rc = __builtin_amdgcn_rcpf(rb(__builtin_amdgcn_sqrtf(rb(rb(var) + 1e-5f))));
        const unsigned dd = pk(bflo(y) - mb, bfhi(y) - mb);
        *(LAS unsigned*)(L + L_XN + i * 256 + lane * 4) = pk(bflo(dd) * rc, bfhi(dd) * rc);
        v4u xa[2];
#pragma unroll
        for (int ks = 0; ks < 2; ++ks) {
          LAS unsigned char* a = L + L_XN + i * 256 + (n & 1) * 128 + ks * 64 + g * 8;
          xa[ks] = cat(*(LAS v2u*)a, *(LAS v2u*)(a + 32));
        }
        PIN(xa[0]); PIN(xa[1]);
        if (w == 4) STAMP(1);
        asm volatile("s_waitcnt vmcnt(0)" : "+v"(ep)::"memory");
        v4u bq[8];
#pragma unroll
        for (int k = 0; k < 8; ++k) { LAS unsigned char* a = base + (((2 * (k >> 1)) ^ fw) << 4) + (k & 1) * 16384; bq[k] = cat(trread(a), trread(a + 8192)); }
        SB();
        f32x4 d[4];
#pragma unroll
        for (int T = 0; T < 4; ++T) d[T] = mfma32(xa[0], bq[2 * T], Z4);
        if (!q_half) {
#pragma unroll
          for (int T = 0; T < 4; ++T) d[T] = mfma32(xa[1], bq[2 * T + 1], d[T]);
        }
        *(LAS float*)(L + L_GU + lane * 16 + i * 4) = sel4(g, d[0][0], d[1][0], d[2][0], d[3][0]);
        *(LAS float*)(L + L_GU + 1024 + lane * 16 + i * 4) = sel4(g, d[0][1], d[1][1], d[2][1], d[3][1]);
        asm volatile("s_waitcnt lgkmcnt(0)" ::: "memory");
        lds_inc(L + L_CTL + C_QP, lane);
        if (i == 0) {
          lds_wait(L + L_CTL + C_QP, 4u);
          const f32x4 s0 = *(LAS f32x4*)(L + L_GU + lane * 32), s1 = *(LAS f32x4*)(L + L_GU + lane * 32 + 16);
          __builtin_amdgcn_raw_buffer_store_b128(v4u{asu((s0[0] + s0[1]) + (s0[2] + s0[3])), ep + 1u, asu((s1[0] + s1[1]) + (s1[2] + s1[3])), ep + 1u}, rs,
                                                 (lane >> 5) * QST + (lane & 31) * 16, WS_QP + x8 * 2 * QST + (s < 12 ? rg * 4096 + cg * 512 : 8192 + rg * 512), SC1);
          STAMP(2);
        }
        __builtin_amdgcn_s_setprio(0);
      } else {
        ARGS(); B0();
        if (w == 4) { STAMP0(); }
      }
      if (w == 7) __builtin_amdgcn_raw_buffer_store_b128(E4, RS(), lane * 16, WS_AP + (grp * 16 + s) * 1024, SC1);
      lds_wait(L + L_CTL + C_KI, 4u);
#pragma unroll 4
      for (int t = 0; t < 16; ++t) { THRW(); VPIECE(t); }
      if (ten) {
        lds_wait(L + L_CTL + C_KR, 2u);
#pragma unroll
        for (int t = 16; t < 20; ++t) { THRW(); VPIECE(t); }
      }
      if (w == 4) STAMP(4);
      VMWAIT(8);
    }
    BARRIER();
    VMWAIT(2);
    BARRIER();
    VMWAIT(0);
    if (w == 4) STAMP(6);
    BARRIER();
    if (!is_wo) RET();
    const __amdgpu_buffer_rsrc_t rs = RS();
    float xv0 = x8 == 0 ? bflo(xres) : 0.f, xv1 = x8 == 0 ? bfhi(xres) : 0.f;
    unsigned sr_ = L_K + n * SST + i * 64 + g * 16, wr_ = L_W + i * 64 + g * 16, gw_ = L_GU + lane * 16 + i * 4, gr_ = L_GU + lane * 32;
    unsigned bo_ = (lane >> 5) * 8192 + (lane & 31) * 4;
    PIN(sr_); PIN(wr_); PIN(gw_); PIN(gr_); PIN(bo_); PIN(xv0); PIN(xv1);
    {
      unsigned sw_ = L_K + ((i >> 1) * 16 + (i & 1) * 4) * SST + g * 256 + pp * 64 + q4 * 16;
      PIN(sw_);
      const unsigned so = WS_AP + (((i >> 1) * 8 + x8) * 16 + 4 * (i & 1)) * 1024;
      v4u v[4];
      _Pragma("nounroll") for (unsigned spin = 0; spin < SPIN_CAP; ++spin) {
        asm volatile("" ::: "memory");
#pragma unroll
        for (int t = 0; t < 4; ++t) v[t] = __builtin_amdgcn_raw_buffer_load_b128(rs, lane * 16 + t * 1024, so, SC1);
        if (spin == 0) { lds_wait(L + L_CTL + C_GT, 2u); continue; }
        if (!__any(umax(umax(wmax(v[0]), wmax(v[1])), umax(wmax(v[2]), wmax(v[3]))) == EMPTY)) break;
      }
#pragma unroll
      for (int t = 0; t < 4; ++t) *(LAS v4u*)(L + sw_ + t * SST) = v[t];
    }
    if (w == 6) STAMP(12);
    BARRIER();
    if (w == 5) STAMP(1);
    v4u of[8];
#pragma unroll
    for (int k = 0; k < 8; ++k) of[k] = *(LAS v4u*)(L + sr_ + (k >> 2) * 16 * SST + (k & 3) * 256);
    const f32x4 wt0 = *(LAS f32x4*)(L + wr_), wt1 = *(LAS f32x4*)(L + wr_ + 256);
    SB();
#pragma unroll
    for (int bb = 0; bb < 2; ++bb) {
      const f32x4 wt = bb ? wt1 : wt0;
      f32x4 d[4] = {Z4, Z4, Z4, Z4};
#pragma unroll
      for (int ks = 0; ks < 4; ++ks)
#pragma unroll
        for (int T = 0; T < 4; ++T) d[T] = mfma32(of[bb * 4 + ks], wf[T * 4 + ks], d[T]);
      float acc[4];
#pragma unroll
      for (int T = 0; T < 4; ++T) acc[T] = fmaf(wt[3], d[T][3], fmaf(wt[2], d[T][2], fmaf(wt[1], d[T][1], wt[0] * d[T][0])));
      *(LAS float*)(L + gw_ + bb * 1024) = sw16(sw32(acc[0], acc[2]), sw32(acc[1], acc[3]));
    }
    if (w == 6) STAMP(2);
    BARRIER();
    const f32x4 s0 = *(LAS f32x4*)(L + gr_), s1 = *(LAS f32x4*)(L + gr_ + 16);
    const unsigned r = ne(pk(((s0[0] + s0[1]) + (s0[2] + s0[3])) + xv0, ((s1[0] + s1[1]) + (s1[2] + s1[3])) + xv1));
    __builtin_amdgcn_raw_buffer_store_b32(r, rs, bo_, WS_BP + 2 * i * 16384 + x8 * 1024 + cg * 128, SC1);
    __builtin_amdgcn_raw_buffer_store_b32(r, rs, bo_, WS_BP + (2 * i + 1) * 16384 + x8 * 1024 + cg * 128, SC1);
    if (w == 4) STAMP(10);
    RET();
  }

  v4u kf[16], kx[8];
  unsigned tag;
  const bool hasx = ten && w < 2;
  const __amdgpu_buffer_rsrc_t rs = RS();
  {
    unsigned vk = g * 256 + ((n ^ g) << 4);
    PIN(vk);
    ARGS(); B0();
    if (!has_q) wait_ticks(t0, KDELAY);
#pragma unroll
    for (int t = 0; t < 8; ++t) { if (t >= THR) THRW(); dma(p.K + kvb, (vk ^ ((t & 3) << 6)) + PIECE(8 * w + t), lb + L_K + w * 16384 + t * 1024); }
    if (nks > 4) {
#pragma unroll
      for (int t = 0; t < 8; ++t) { THRW(); dma(p.K + kvb, (vk ^ ((t & 3) << 6)) + PIECE(32 + 8 * w + t), lb + L_K + w * 16384 + 8192 + t * 1024); }
    }
    if (hasx) {
#pragma unroll
      for (int t = 0; t < 8; ++t) dma(p.K + kvb, (vk ^ ((t & 3) << 6)) + PIECE(64 + 8 * w + t), lb + L_V + 65536 + w * 8192 + t * 1024);
    }
    lds_inc(L + L_CTL + C_KI, lane);
    __builtin_amdgcn_s_setprio(2);
    const unsigned qo = WS_QP + (x8 * 2 + b) * QST;
    const bool q4p = (unsigned)(2 * w + (lane >> 5)) == ((3u - (unsigned)((size_t)p.wq >> 7)) & 7u);
    const unsigned qx = q4p ? (lane & 31) * 16 + 8192 : 0x80000000u;
    asm volatile("s_load_dword %0, %1, 0x0\n\ts_waitcnt lgkmcnt(0)" : "=s"(tag) : "s"(p.ws) : "memory");
    tag += 1u;
    wait_ticks(t0, QDELAY);
    if (w == 1) STAMP(15);
#define QLOADS() v0 = __builtin_amdgcn_raw_buffer_load_b128(rs, lane * 16, qo + w * 1024, SC1); v1 = __builtin_amdgcn_raw_buffer_load_b128(rs, lane * 16, qo + 4096 + w * 1024, SC1); \
    v2 = __builtin_amdgcn_raw_buffer_load_b128(rs, qx, qo, SC1); v3 = __builtin_amdgcn_raw_buffer_load_b128(rs, qx, qo + 512, SC1)
    v4u v0, v1, v2, v3;
    QLOADS();
    VMWAIT(4);
#pragma unroll
    for (int k = 0; k < 8; ++k) kx[k] = *(LAS v4u*)(L + L_V + 65536 + (w & 1) * 8192 + n * 256 + (k >> 2) * 4096 + (((4 * (k & 3) + g) ^ n) << 4));
    asm volatile("s_waitcnt lgkmcnt(0)" : "+v"(kx[0]), "+v"(kx[1]), "+v"(kx[2]), "+v"(kx[3]), "+v"(kx[4]), "+v"(kx[5]), "+v"(kx[6]), "+v"(kx[7])::"memory");
    if (w < 2) lds_inc(L + L_CTL + C_KR, lane);
#pragma unroll
    for (int k = 0; k < 16; ++k) kf[k] = *(LAS v4u*)(L + L_K + w * 16384 + n * 256 + (k >> 2) * 4096 + (((4 * (k & 3) + g) ^ n) << 4));
    asm volatile("s_waitcnt lgkmcnt(0)" : "+v"(kf[0]), "+v"(kf[1]), "+v"(kf[2]), "+v"(kf[3]), "+v"(kf[4]), "+v"(kf[5]), "+v"(kf[6]), "+v"(kf[7]),
                 "+v"(kf[8]), "+v"(kf[9]), "+v"(kf[10]), "+v"(kf[11]), "+v"(kf[12]), "+v"(kf[13]), "+v"(kf[14]), "+v"(kf[15])::"memory");
    if (w == 1) STAMP(8);
    _Pragma("nounroll") for (unsigned spin = 0; spin < SPIN_CAP; ++spin) {
      if (!__any(((tagdiff(v0, tag) | tagdiff(v1, tag)) | (q4p ? tagdiff(v2, tag) | tagdiff(v3, tag) : 0u)) != 0u)) break;
      asm volatile("" ::: "memory");
      QLOADS();
    }
    *(LAS unsigned*)(L + L_Q + w * 272 + lane * 4) = pk((asf(v0.x) + asf(v2.x)) + (asf(v1.x) + asf(v3.x)), (asf(v0.z) + asf(v2.z)) + (asf(v1.z) + asf(v3.z)));
    asm volatile("s_waitcnt lgkmcnt(0)" ::: "memory");
    lds_inc(L + L_CTL + C_QR, lane);
    if (w == 0) STAMP(3);
  }
  lds_wait(L + L_CTL + C_QR, 4u);
  float mw, ls;
  {
    v4u qf[4];
#pragma unroll
    for (int ks = 0; ks < 4; ++ks) qf[ks] = *(LAS v4u*)(L + L_Q + pp * 272 + (4 * ks + g) * 16);
    f32x4 d[4];
#pragma unroll
    for (int u = 0; u < 4; ++u) {
      d[u] = Z4;
#pragma unroll
      for (int ks = 0; ks < 4; ++ks) d[u] = mfma32(kf[u * 4 + ks], qf[ks], d[u]);
    }
    float sc[4], sx[4];
#pragma unroll
    for (int r = 0; r < 4; ++r) { sc[r] = sel4(q4, d[0][r], d[1][r], d[2][r], d[3][r]); sx[r] = -__builtin_inff(); }
    round_scores(sc);
    if (nks == 4) {
#pragma unroll
      for (int r = 0; r < 4; ++r) sc[r] = q4 < 2 ? sc[r] : -__builtin_inff();
    }
    if (hasx) {
      f32x4 e0 = Z4, e1 = Z4;
#pragma unroll
      for (int ks = 0; ks < 4; ++ks) { e0 = mfma32(kx[ks], qf[ks], e0); e1 = mfma32(kx[4 + ks], qf[ks], e1); }
#pragma unroll
      for (int r = 0; r < 4; ++r) sx[r] = (q4 & 1) ? e1[r] : e0[r];
      round_scores(sx);
    }
    mw = max_mod4(fmaxf(fmaxf(fmaxf(sc[0], sc[1]), fmaxf(sc[2], sc[3])), fmaxf(fmaxf(sx[0], sx[1]), fmaxf(sx[2], sx[3]))));
#pragma unroll
    for (int r = 0; r < 4; ++r) { sc[r] = __builtin_amdgcn_exp2f((sc[r] - mw) * LOG2E); sx[r] = __builtin_amdgcn_exp2f((sx[r] - mw) * LOG2E); }
    *(LAS v2u*)(L + L_P + pp * PST + (32 * w + 128 * (q4 >> 1) + 16 * (q4 & 1) + 4 * g) * 2) = v2u{pk(sc[0], sc[1]), pk(sc[2], sc[3])};
    if (q4 < 2 && w < 2) *(LAS v2u*)(L + L_P + pp * PST + (256 + 32 * w + 16 * q4 + 4 * g) * 2) = v2u{pk(sx[0], sx[1]), pk(sx[2], sx[3])};
    ls = sum_mod4(((sc[0] + sc[1]) + (sc[2] + sc[3])) + 0.5f * ((sx[0] + sx[1]) + (sx[2] + sx[3])));
    if (lane < 4) { *(LAS float*)(L + L_MX + lane * 16 + w * 4) = mw; *(LAS float*)(L + L_LS + lane * 16 + w * 4) = ls; }
  }
  if (w == 0) STAMP(5);
  BARRIER();
  v4u ml;
  {
    LAS unsigned char* pb = L + L_P + pp * PST + g * 16;
    const unsigned f0 = (q4 << 2) | ((2 * g) & 3), c0 = 4 * w + (pp >> 1), vb = (8 * g + q4) * 256 + 8 * (pp & 1);
    const unsigned v00 = vb + ((c0 ^ f0) << 4), v01 = vb + 1024 + ((c0 ^ f0 ^ 1) << 4), v10 = vb + ((c0 ^ f0 ^ 2) << 4), v11 = vb + 1024 + ((c0 ^ f0 ^ 3) << 4);
    const f32x4 m4 = *(LAS f32x4*)(L + L_MX + pp * 16);
    const float m = fmaxf(fmaxf(m4[0], m4[1]), fmaxf(m4[2], m4[3]));
    float fs[4];
#pragma unroll
    for (int k = 0; k < 4; ++k) fs[k] = __builtin_amdgcn_exp2f((m4[k] - m) * LOG2E);
    if (w == 3) {
      const f32x4 l4 = *(LAS f32x4*)(L + L_LS + pp * 16);
      const float l = fmaf(l4[3], fs[3], fmaf(l4[2], fs[2], fmaf(l4[1], fs[1], l4[0] * fs[0])));
      if (lane < 4) __builtin_amdgcn_raw_buffer_store_b128(v4u{asu(m), tag, asu(l), tag}, rs, lane * 16, WS_ML + grp * 2048 + s * 128, SC1);
    }
    f32x4 o0 = Z4, o1 = Z4;
    if (nks > 4) {
      v4u pf[4], va[4], vc[4];
#pragma unroll
      for (int ks = 0; ks < 4; ++ks) {
        LAS unsigned char* vk = L + L_V + ks * 8192;
        pf[ks] = *(LAS v4u*)(pb + ks * 64); va[ks] = cat(trread(vk + v00), trread(vk + v01)); vc[ks] = cat(trread(vk + v10), trread(vk + v11));
      }
      SB();
      f32x4 A[4], B[4];
#pragma unroll
      for (int ks = 0; ks < 4; ++ks) { A[ks] = mfma32(va[ks], pf[ks], Z4); B[ks] = mfma32(vc[ks], pf[ks], Z4); }
      if (ten) {
#pragma unroll
        for (int ks = 0; ks < 2; ++ks) {
          LAS unsigned char* vk = L + L_V + (4 + ks) * 8192;
          const v4u pg = *(LAS v4u*)(pb + (4 + ks) * 64);
          A[ks] = mfma32(cat(trread(vk + v00), trread(vk + v01)), pg, A[ks]); B[ks] = mfma32(cat(trread(vk + v10), trread(vk + v11)), pg, B[ks]);
        }
      }
      o0 = A[0] * fs[0] + A[1] * fs[1] + A[2] * fs[2] + A[3] * fs[3]; o1 = B[0] * fs[0] + B[1] * fs[1] + B[2] * fs[2] + B[3] * fs[3];
    }
    float fl0 = ten ? fs[2] : fs[0], fl1 = ten ? fs[3] : fs[1], fl2 = ten ? fs[0] : fs[2], fl3 = ten ? fs[1] : fs[3];
    const unsigned lastv = L_V + (nks - 4) * 8192;
    LAS unsigned char* pl = pb + (nks - 4) * 64;
    v4u pl0 = *(LAS v4u*)pl, pl1 = *(LAS v4u*)(pl + 64), pl2 = *(LAS v4u*)(pl + 128), pl3 = *(LAS v4u*)(pl + 192);
    unsigned a00 = lastv + v00, a01 = lastv + v01, a10 = lastv + v10, a11 = lastv + v11, ao_ = n < 4 ? g * 64 + n * 16 : 0x80000000u;
    PIN(a00); PIN(a01); PIN(a10); PIN(a11); PIN(ao_); PIN(pl0); PIN(pl1); PIN(pl2); PIN(pl3); PIN(o0); PIN(o1); PIN(fl0); PIN(fl1); PIN(fl2); PIN(fl3);
    BARRIER();
    ml = __builtin_amdgcn_raw_buffer_load_b128(rs, (is_wo && w < 2) ? (lane < 56 ? lane >> 2 : 0) * 128 + pp * 16 : 0x80000000u, WS_ML + (w * 8 + x8) * 2048, SC1);
    {
      v4u va[3], vc[3];
#pragma unroll
      for (int ks = 0; ks < 3; ++ks) {
        va[ks] = cat(trread(L + a00 + ks * 8192), trread(L + a01 + ks * 8192)); vc[ks] = cat(trread(L + a10 + ks * 8192), trread(L + a11 + ks * 8192));
      }
      SB();
      const f32x4 e0 = mfma32(va[0], pl0, Z4), e1 = mfma32(vc[0], pl0, Z4), e2 = mfma32(va[1], pl1, Z4), e3 = mfma32(vc[1], pl1, Z4);
      const f32x4 e4 = mfma32(va[2], pl2, Z4), e5 = mfma32(vc[2], pl2, Z4);
      o0 += e0 * fl0 + e2 * fl1 + e4 * fl2; o1 += e1 * fl0 + e3 * fl1 + e5 * fl2;
    }
    PIN(o0); PIN(o1);
    BARRIER();
    const v4u va3 = cat(trread(L + a00 + 3 * 8192), trread(L + a01 + 3 * 8192)), vc3 = cat(trread(L + a10 + 3 * 8192), trread(L + a11 + 3 * 8192));
    SB();
    o0 += mfma32(va3, pl3, Z4) * fl3; o1 += mfma32(vc3, pl3, Z4) * fl3;
    __builtin_amdgcn_raw_buffer_store_b128(v4u{ne(pk(o0[0], o0[1])), ne(pk(o0[2], o0[3])), ne(pk(o1[0], o1[1])), ne(pk(o1[2], o1[3]))}, rs,
                                             ao_, WS_AP + (grp * 16 + s) * 1024 + w * 256, SC1);
  }
  if (w == 0) STAMP(7);
  if (!is_wo && s != 12) RET();
  if (is_wo) {
    unsigned sw_ = L_K + ((w >> 1) * 16 + 8 + (w & 1) * 3) * SST + g * 256 + pp * 64 + q4 * 16;
    PIN(sw_);
    const unsigned so = WS_AP + (((w >> 1) * 8 + x8) * 16 + 8 + 3 * (w & 1)) * 1024;
    v4u v[3];
    if (w < 2) {
      _Pragma("nounroll") for (unsigned spin = 0; spin < SPIN_CAP; ++spin) {
        if (!__any(tagdiff(ml, tag) != 0u)) break;
        __builtin_amdgcn_s_sleep(2);
        asm volatile("" ::: "memory");
        ml = __builtin_amdgcn_raw_buffer_load_b128(rs, (lane < 56 ? lane >> 2 : 0) * 128 + pp * 16, WS_ML + (w * 8 + x8) * 2048, SC1);
      }
      lds_inc(L + L_CTL + C_GT, lane);
      if (w == 1) STAMP(14);
    }
    lds_wait(L + L_CTL + C_GT, 2u);
#pragma unroll
    for (int t = 0; t < 3; ++t) v[t] = __builtin_amdgcn_raw_buffer_load_b128(rs, lane * 16 + t * 1024, so, SC1);
    asm volatile("" ::: "memory");
    if (w < 2) {
      const float M = max_mod4(asf(ml.x));
      const float e = lane < 56 ? __builtin_amdgcn_exp2f((asf(ml.x) - M) * LOG2E) : 0.f;
      *(LAS float*)(L + L_W + w * 256 + pp * 64 + (lane >> 2) * 4) = e * __builtin_amdgcn_rcpf(sum_mod4(e * asf(ml.z)));
    }
    _Pragma("nounroll") for (unsigned spin = 0; spin < SPIN_CAP; ++spin) {
      if (!__any(umax(umax(wmax(v[0]), wmax(v[1])), wmax(v[2])) == EMPTY)) break;
      asm volatile("" ::: "memory");
#pragma unroll
      for (int t = 0; t < 3; ++t) v[t] = __builtin_amdgcn_raw_buffer_load_b128(rs, lane * 16 + t * 1024, so, SC1);
    }
#pragma unroll
    for (int t = 0; t < 3; ++t) *(LAS v4u*)(L + sw_ + t * SST) = v[t];
    *(LAS v4u*)(L + sw_ + (6 - 2 * (w & 1)) * SST) = v[0];
    if (w == 0) STAMP(9);
    BARRIER();
    RET();
  }
  {
    const int col = (grp >> 1) * 64 + (w & 1) * 32 + ((grp & 1) * 2 + (w >> 1)) * 4;
    _Pragma("nounroll") for (unsigned spin = 0; spin < SPIN_CAP; ++spin) {
      asm volatile("" ::: "memory");
      if (!__any(flagdiff(__builtin_amdgcn_raw_buffer_load_b128(rs, (lane < 46 ? lane : 0) * 16, WS_FL, SC1), tag) != 0u)) break;
      __builtin_amdgcn_s_sleep(4);
    }
    float f[8];
    {
      v2u h0, h1;
      const unsigned vo = (lane & 1) * 8192 + ((lane >> 1) & 7) * 1024, so = WS_BP + x8 * 16384 + col * 2;
      _Pragma("nounroll") for (unsigned spin = 0; spin < SPIN_CAP; ++spin) {
        asm volatile("" ::: "memory");
        h0 = __builtin_amdgcn_raw_buffer_load_b64(rs, vo, so, SC1);
        h1 = __builtin_amdgcn_raw_buffer_load_b64(rs, vo + 32, so, SC1);
        if (!__any(umax(umax(h0.x, h0.y), umax(h1.x, h1.y)) == EMPTY)) break;
        __builtin_amdgcn_s_sleep(4);
      }
      if (lane >= 16) { h0 = v2u{0u, 0u}; h1 = v2u{0u, 0u}; }
      f[0] = bflo(h0.x); f[1] = bfhi(h0.x); f[2] = bflo(h0.y); f[3] = bfhi(h0.y);
      f[4] = bflo(h1.x); f[5] = bfhi(h1.x); f[6] = bflo(h1.y); f[7] = bfhi(h1.y);
    }
    unsigned oo_ = ((lane & 1) * 512 + col + (lane >> 5) * 16 + ((lane >> 3) & 3)) * 2, msk = lane < 56 ? ~0u : 0u;
    PIN(oo_); PIN(msk);
#pragma unroll
    for (int k = 0; k < 8; ++k) PIN(f[k]);
    v4u v[1];
    gather_dense<1>(rs, (lane < 56 ? (lane >> 1) * 128 : 0) + (lane & 1) * 16, WS_CP + grp * 4096 + w * 32, v);
#pragma unroll
    for (int k = 0; k < 4; ++k) { const unsigned t = v[0][k] & msk; f[2 * k] += bflo(t); f[2 * k + 1] += bfhi(t); }
    const float u0 = sw32(f[0], f[4]), u1 = sw32(f[1], f[5]), u2 = sw32(f[2], f[6]), u3 = sw32(f[3], f[7]);
    float y0 = sw16(u0, u2), y1 = sw16(u1, u3);
    y0 += dpp<ROR8>(y0); y1 += dpp<ROR8>(y1);
    float y = (lane & 8) ? y1 : y0;
    y += dpp<XOR2>(y);
    y += dpp<HMIR>(dpp<XOR1>(y));
    if ((lane & 6) == 0) *(GAS u16*)((GAS unsigned char*)p.out + oo_) = (u16)pk(y, y);
    if (c == 96 && tid == 0) __builtin_amdgcn_raw_buffer_store_b32(tag, rs, 0, 0, SC1);
    if (w == 0) STAMP(13);
  }
  RET();
}

extern "C" void kernel_launch(void* const* d_in, const int* in_sizes, int n_in, void* d_out, int out_size, void* d_ws, size_t ws_size,
                              hipStream_t stream) {
  Params p{(const u16*)d_in[0], (const u16*)d_in[1], (const u16*)d_in[2], (const u16*)d_in[3], (const u16*)d_in[4],
           (const u16*)d_in[6], (const u16*)d_in[7], (const u16*)d_in[8], (u16*)d_out, (unsigned char*)d_ws};
  (void)hipFuncSetAttribute((const void*)fused_decode, hipFuncAttributeMaxDynamicSharedMemorySize, (int)L_END + LDS_EXTRA);
  fused_decode<<<256, 512, L_END + LDS_EXTRA, stream>>>(p);
}
```
